```python
import torch

def _softmax(x, axis=-1):
    return torch.softmax(x, dim=axis)
def _gelu(x):
    return torch.nn.functional.gelu(x, approximate="tanh")
def _silu(x):
    return torch.nn.functional.silu(x)
def _sigmoid(x):
    return torch.sigmoid(x)
def _layernorm(x, axis=-1, eps=1e-5):
    m = x.mean(dim=axis, keepdim=True)
    v = x.var(dim=axis, keepdim=True, unbiased=False)
    return (x - m) / torch.sqrt(v + eps)
def _scatter_add(tgt, idx, src, axis=0):
    return tgt.clone().index_add_(axis, idx, src)
def _segment_sum(src, seg, n):
    out = torch.zeros((n,) + src.shape[1:], dtype=src.dtype)
    return out.index_add_(0, seg, src)
def _pad(x, pw):
    flat = tuple(v for pair in reversed(pw) for v in pair)
    return torch.nn.functional.pad(x, flat)

kernel_name = 'concat_heads__mha_block__spectral_gfnet__prompt'

def setup_inputs():

    _w = (lambda a: (lambda t: t.to(torch.bfloat16) if t.is_floating_point() else t)(torch.as_tensor(a)))
    def _A_setup():
        B, S, D, H = 1, 2048, 448, 16
        return {
            "x": _w((lambda *s: torch.randn(*s, dtype=torch.bfloat16) * 0.2)(B, S, D)),
            "wqkv": _w((lambda *s: torch.randn(*s, dtype=torch.bfloat16) * 0.2)(D, 3 * D)),
            "wo": _w((lambda *s: torch.randn(*s, dtype=torch.bfloat16) * 0.05)(D, D)),
        }
    _A_in = _A_setup()

    def _B_setup():
        B, S, D = 1, 2048, 448
        Df = D // 2 + 1
        return {
            "x": _w((lambda *s: torch.randn(*s, dtype=torch.bfloat16) * 0.05)(B, S, D)),
            "w_r": _w((lambda *s: torch.randn(*s, dtype=torch.bfloat16) * 6.4)(S, Df) * 0.02),
            "w_i": _w((lambda *s: torch.randn(*s, dtype=torch.bfloat16) * 6.4)(S, Df) * 0.02),
        }
    _B_in = _B_setup()

    return {
        "A_x": _A_in["x"],
        "A_wqkv": _A_in["wqkv"],
        "A_wo": _A_in["wo"],
        "B_x": _B_in["x"],
        "B_w_r": _B_in["w_r"],
        "B_w_i": _B_in["w_i"],
    }

def reference(A_x, A_wqkv, A_wo, B_x, B_w_r, B_w_i):
    def _A_ref(x, wqkv, wo):
        B, S, D = x.shape
        H = 16
        Dh = D // H
        qkv = (lambda x, *d: x.permute(*d))((x @ wqkv).reshape(B, S, 3, H, Dh), 2, 0, 3, 1, 4)
        q, k, v = qkv[0], qkv[1], qkv[2]
        scores = torch.einsum("bhqd,bhkd->bhqk", q, k) * (Dh ** -0.5)
        p = _softmax(scores, axis=-1)
        o = torch.einsum("bhqk,bhkd->bhqd", p, v)
        return (lambda x, *d: x.permute(*d))(o, 0, 2, 1, 3).reshape(B, S, D) @ wo

    def _B_ref(x, w_r, w_i):
        B, S, D = x.shape
        X = (lambda x, axes=(-2,-1): torch.fft.rfft2(x.float(), dim=axes))(x, axes=(-2, -1))
        W = w_r + 1j * w_i
        X = X * W
        y = (lambda x, s, axes=(-2,-1): torch.fft.irfft2(x, s=s, dim=axes).to(torch.bfloat16))(X, (S, D), axes=(-2, -1))
        return y

    a = _A_ref(A_x, A_wqkv, A_wo)
    b = _B_ref(B_x, B_w_r, B_w_i)
    return torch.concatenate([a, b], axis=-1)
```

```cpp
#include <hip/hip_runtime.h>
#include <hip/hip_bf16.h>
#include <stdint.h>

typedef __attribute__((ext_vector_type(8))) short bf16x8;
typedef __attribute__((ext_vector_type(4))) short s16x4;
typedef __attribute__((ext_vector_type(16))) float f32x16;
typedef __attribute__((ext_vector_type(4))) float f32x4;
typedef __attribute__((ext_vector_type(4))) unsigned u32x4;
typedef __attribute__((ext_vector_type(2))) unsigned u32x2;
typedef __attribute__((ext_vector_type(2))) float cf;
#define LAS __attribute__((address_space(3)))
#define DEVI __device__ __forceinline__
#define MFMA32(a, b, c) __builtin_amdgcn_mfma_f32_32x32x16_bf16(a, b, c, 0, 0, 0)
#define MFMA16(a, b, c) __builtin_amdgcn_mfma_f32_16x16x32_bf16(a, b, c, 0, 0, 0)

constexpr int NQ = 1800;
constexpr size_t WS_QF = 0;
constexpr size_t WS_KV = WS_QF + (size_t)16 * 64 * 2 * 1024;
constexpr size_t WS_WO = WS_KV + (size_t)16 * 64 * 4 * 1024;
constexpr size_t WS_OF = WS_WO + (size_t)28 * 16 * 1024;
constexpr size_t WS_Y1 = WS_OF + (size_t)64 * 16 * 2 * 1024;
constexpr size_t WS_Y2 = WS_Y1 + (size_t)256 * NQ * 8;
constexpr size_t WS_END = WS_Y2 + (size_t)256 * NQ * 8;
constexpr float QSCALE = 0.18898223650461362f * 1.4426950408889634f;

typedef __attribute__((ext_vector_type(2))) __bf16 bf16x2_t;
DEVI unsigned cvtpk(float lo, float hi) { return __builtin_bit_cast(unsigned, __builtin_convertvector(cf{lo, hi}, bf16x2_t)); }
DEVI float bflo(unsigned u) { return __uint_as_float(u << 16); }
DEVI float bfhi(unsigned u) { return __uint_as_float(u & 0xffff0000u); }
DEVI void glds16(const void* g, unsigned lds_base) {
    unsigned sv;
    asm volatile("s_mov_b32 %0, m0\n\ts_mov_b32 m0, %2\n\ts_nop 0\n\tglobal_load_lds_dwordx4 %1, off\n\ts_mov_b32 m0, %0"
                 : "=&s"(sv) : "v"(g), "s"(__builtin_amdgcn_readfirstlane(lds_base)) : "memory");
}
#define WAITV(N) asm volatile("s_waitcnt vmcnt(" #N ")" ::: "memory")
#define WAITL() asm volatile("s_waitcnt lgkmcnt(0)" ::: "memory")
#define BAR() asm volatile("s_waitcnt lgkmcnt(0)\n\ts_barrier" ::: "memory")

template <int DIR> DEVI cf cmul(cf a, cf w) {
    cf t, r;
    asm("v_pk_mul_f32 %0, %1, %2 op_sel:[1,0] op_sel_hi:[0,0]" : "=v"(t) : "v"(a), "v"(w));
    if (DIR > 0) asm("v_pk_fma_f32 %0, %1, %2, %3 op_sel:[1,0,0] op_sel_hi:[1,1,1] neg_lo:[0,0,1]" : "=v"(r) : "v"(w), "v"(a), "v"(t));
    else asm("v_pk_fma_f32 %0, %1, %2, %3 op_sel:[1,0,0] op_sel_hi:[1,1,1] neg_hi:[0,0,1]" : "=v"(r) : "v"(w), "v"(a), "v"(t));
    return r;
}
template <int DIR> DEVI cf addrot(cf t, cf x) {
    cf r;
    if (DIR > 0) asm("v_pk_add_f32 %0, %1, %2 op_sel:[1,0] op_sel_hi:[0,1] neg_lo:[1,0]" : "=v"(r) : "v"(x), "v"(t));
    else asm("v_pk_add_f32 %0, %1, %2 op_sel:[1,0] op_sel_hi:[0,1] neg_hi:[1,0]" : "=v"(r) : "v"(x), "v"(t));
    return r;
}
DEVI cf cconj(cf a) { return cf{a.x, -a.y}; }
DEVI cf tw(int num, float inv_den) {
    const float x = (float)num * inv_den;
    cf w = cf{__builtin_amdgcn_sinf(x), __builtin_amdgcn_cosf(x)};
    asm("s_nop 0" : "+v"(w));
    return w;
}
template <int DIR> DEVI void dft4(cf& a0, cf& a1, cf& a2, cf& a3) {
    cf t0 = a0 + a2, t1 = a0 - a2, t2 = a1 + a3, t3 = a1 - a3;
    a0 = t0 + t2; a2 = t0 - t2; a1 = addrot<DIR>(t1, t3); a3 = addrot<-DIR>(t1, t3);
}
template <int DIR> DEVI void dft8(cf* a) {
    cf e0 = a[0], e1 = a[2], e2 = a[4], e3 = a[6], o0 = a[1], o1 = a[3], o2 = a[5], o3 = a[7];
    dft4<DIR>(e0, e1, e2, e3); dft4<DIR>(o0, o1, o2, o3);
    const float r = 0.70710678118654752f;
    o1 = addrot<DIR>(o1, o1); o3 = addrot<-DIR>(o3, o3);
    a[0] = e0 + o0; a[4] = e0 - o0; a[1] = e1 + o1 * r; a[5] = e1 - o1 * r;
    a[2] = addrot<DIR>(e2, o2); a[6] = addrot<-DIR>(e2, o2); a[3] = e3 - o3 * r; a[7] = e3 + o3 * r;
}
template <int DIR> DEVI void dft7(cf* a) {
    const float c1 = 0.62348980185873353f, c2 = -0.22252093395631440f, c3 = -0.90096886790241913f;
    const float s1 = 0.78183148246802981f, s2 = 0.97492791218182361f, s3 = 0.43388373911755812f;
    cf t1 = a[1] + a[6], t2 = a[2] + a[5], t3 = a[3] + a[4], u1 = a[1] - a[6], u2 = a[2] - a[5], u3 = a[3] - a[4];
    cf A1 = a[0] + t1 * c1 + t2 * c2 + t3 * c3, A2 = a[0] + t1 * c2 + t2 * c3 + t3 * c1, A3 = a[0] + t1 * c3 + t2 * c1 + t3 * c2;
    cf B1 = u1 * s1 + u2 * s2 + u3 * s3, B2 = u1 * s2 - u2 * s3 - u3 * s1, B3 = u1 * s3 - u2 * s1 + u3 * s2;
    a[0] = a[0] + t1 + t2 + t3;
    a[1] = addrot<DIR>(A1, B1); a[6] = addrot<-DIR>(A1, B1); a[2] = addrot<DIR>(A2, B2); a[5] = addrot<-DIR>(A2, B2);
    a[3] = addrot<DIR>(A3, B3); a[4] = addrot<-DIR>(A3, B3);
}
constexpr int RSTR = 72;
constexpr int RFB = 7 * RSTR * 8;

constexpr int K1_W = 0, K1_X = 448 * 192, K1_F = K1_X + 4 * 14336, K1_LDS = K1_F + 4 * RFB;

__global__ void __launch_bounds__(512, 2) k1_kernel(const unsigned short* __restrict__ x, const unsigned short* __restrict__ wqkv,
                                                    const unsigned short* __restrict__ wo, const unsigned short* __restrict__ bx,
                                                    char* __restrict__ ws) {
    extern __shared__ __attribute__((aligned(16))) char lds[];
    const int tid = threadIdx.x, lane = tid & 63, wid = __builtin_amdgcn_readfirstlane(tid >> 6);
    const int bid = blockIdx.x;
    const unsigned lds0 = (unsigned)(uintptr_t)lds;
    if (wid < 4) {
        const int xcd = bid & 7, o = bid >> 3;
        const int ti = (xcd >> 1) * 4 + (o & 3), nt = (xcd & 1) * 8 + (o >> 2);
        const int base16 = (168 * nt) & ~15, off8 = (168 * nt) & 15;
        const char* wsrc = (const char*)wqkv + base16;
        const char* xsrc = (const char*)x + (size_t)(ti * 128 + wid * 32) * 896;
        auto ldw = [&](int i) {
            int c = i * 64 + lane, row = c / 12, col = c - row * 12; col = col > 10 ? 10 : col;
            glds16(wsrc + row * 2688 + col * 16, lds0 + K1_W + i * 1024);
        };
        auto ldx = [&](int i, int half) {
            int c = i * 64 + lane, row = c / 28, col = c - row * 28; col ^= (row >> 2) & 3;
            glds16(xsrc + row * 896 + half * 448 + col * 16, lds0 + K1_X + wid * 14336 + i * 1024);
        };
#pragma unroll
        for (int i = 0; i < 11; ++i) { int p = wid + 4 * i; ldw(p < 42 ? p : 41); }
#pragma unroll
        for (int i = 0; i < 14; ++i) ldx(i, 0);
#pragma unroll
        for (int i = 0; i < 11; ++i) { int p = 42 + wid + 4 * i; ldw(p < 84 ? p : 83); }
        const int r = lane & 31, h = lane >> 5;
        const LAS char* xr = (const LAS char*)lds + K1_X + wid * 14336 + r * 448;
        const int sw = (r >> 2) & 3;
        const LAS char* wr = (const LAS char*)lds + K1_W + (8 * h + ((lane & 15) >> 2)) * 192 + off8 + ((lane >> 4) & 1) * 32 + (lane & 3) * 8;
        f32x16 acc[3];
#pragma unroll
        for (int j = 0; j < 3; ++j) acc[j] = f32x16{};
        const int s0 = 3 * nt;
        bf16x8 xf[14];
        WAITV(11); BAR();
#pragma unroll
        for (int half = 0; half < 2; ++half) {
#pragma unroll
            for (int kk = 0; kk < 14; ++kk) xf[kk] = *(const LAS bf16x8*)(xr + (((2 * kk + h) ^ sw) * 16));
            if (half == 0) {
                WAITL();
#pragma unroll
                for (int kk = 0; kk < 14; ++kk) asm volatile("" : "+v"(xf[kk]));
#pragma unroll
                for (int i = 0; i < 14; ++i) ldx(i, 1);
            }
#pragma unroll
            for (int j = 0; j < 3; ++j) {
                const bool isv = (s0 + j) >= 32;
#pragma unroll
                for (int kk = 0; kk < 14; ++kk) {
                    const LAS char* p = wr + (half * 224 + kk * 16) * 192 + j * 56;
                    s16x4 lo = __builtin_amdgcn_ds_read_tr16_b64_v4i16((LAS s16x4*)p);
                    s16x4 hi = __builtin_amdgcn_ds_read_tr16_b64_v4i16((LAS s16x4*)(p + 4 * 192));
                    bf16x8 wf = __builtin_shufflevector(lo, hi, 0, 1, 2, 3, 4, 5, 6, 7);
                    if (isv) acc[j] = MFMA32(xf[kk], wf, acc[j]);
                    else acc[j] = MFMA32(wf, xf[kk], acc[j]);
                }
            }
            if (half == 0) { WAITV(0); BAR(); }
        }
        const int tile = ti * 4 + wid;
#pragma unroll
        for (int j = 0; j < 3; ++j) {
            const int s = s0 + j, kind = s >> 4, head = s & 15;
            f32x16 a = acc[j];
            if (kind == 0) a = a * QSCALE;
            u32x4 f0, f1;
#pragma unroll
            for (int e = 0; e < 4; ++e) { f0[e] = cvtpk(a[2 * e], a[2 * e + 1]); f1[e] = cvtpk(a[8 + 2 * e], a[9 + 2 * e]); }
            u32x4* dst;
            if (kind == 2) {
                if (r >= 28) { unsigned v = r == 28 ? 0x3f803f80u : 0u; f0 = u32x4{v, v, v, v}; f1 = f0; }
                dst = (u32x4*)(ws + WS_KV) + ((head * 64 + tile) * 4 + 2) * 64 + lane;
            } else if (kind == 1) {
                if (h) { f1[2] = 0x3f803f80u; f1[3] = 0x00003f80u; }
                dst = (u32x4*)(ws + WS_KV) + ((head * 64 + tile) * 4) * 64 + lane;
            } else {
                if (h) { f1[2] = 0u; f1[3] = 0u; }
                dst = (u32x4*)(ws + WS_QF) + ((head * 64 + tile) * 2) * 64 + lane;
            }
            dst[0] = f0; dst[64] = f1;
        }
    } else {
        const int b = bid, t = tid - 256, w = wid - 4;
        LAS cf* fb = (LAS cf*)(lds + K1_F);
        if (t < 224) {
            unsigned xv[8];
#pragma unroll
            for (int a = 0; a < 8; ++a) xv[a] = *(const unsigned*)(bx + (size_t)(256 * a + b) * 448 + 2 * t);
            cf v[8];
#pragma unroll
            for (int a = 0; a < 8; ++a) v[a] = cf{bflo(xv[a]), bfhi(xv[a])};
            const float rr = 0.70710678118654752f;
            cf p0 = v[0] + v[4], m0 = v[0] - v[4], p1 = v[1] + v[5], m1 = v[1] - v[5];
            cf p2 = v[2] + v[6], m2 = v[2] - v[6], p3 = v[3] + v[7], m3 = v[3] - v[7];
            cf e0 = p0 + p2, e1 = p1 + p3;
            cf z0 = e0 + e1, z4 = e0 - e1;
            cf z2r = p0 - p2, z2i = p3 - p1;
            cf qa = (m1 - m3) * rr, qb = (m1 + m3) * rr;
            cf z1r = m0 + qa, z1i = -m2 - qb, z3r = m0 - qa, z3i = m2 - qb;
            cf w1 = tw(b, 1.f / 2048), w2 = tw(2 * b, 1.f / 2048), w3 = tw(3 * b, 1.f / 2048);
            const int n2 = 2 * t, pos = (n2 >> 6) * RSTR + (n2 & 63);
            cf a0, a1;
            *(LAS f32x4*)(fb + pos) = f32x4{z0.x, z4.x, z0.y, z4.y};
            a0 = cmul<-1>(cf{z1r.x, z1i.x}, w1); a1 = cmul<-1>(cf{z1r.y, z1i.y}, w1);
            *(LAS f32x4*)(fb + 7 * RSTR + pos) = f32x4{a0.x, a0.y, a1.x, a1.y};
            a0 = cmul<-1>(cf{z2r.x, z2i.x}, w2); a1 = cmul<-1>(cf{z2r.y, z2i.y}, w2);
            *(LAS f32x4*)(fb + 14 * RSTR + pos) = f32x4{a0.x, a0.y, a1.x, a1.y};
            a0 = cmul<-1>(cf{z3r.x, z3i.x}, w3); a1 = cmul<-1>(cf{z3r.y, z3i.y}, w3);
            *(LAS f32x4*)(fb + 21 * RSTR + pos) = f32x4{a0.x, a0.y, a1.x, a1.y};
        }
        if (t < 112) {
            const int item = bid * 112 + t, l = item & 63, fr = item >> 6, head = fr & 15, cb = fr >> 4;
            const int g = l >> 4, d0 = 16 * (g >> 1) + 4 * (g & 1);
            const unsigned short* src = wo + (size_t)(head * 28) * 448 + cb * 16 + (l & 15);
            unsigned v[8];
#pragma unroll
            for (int j = 0; j < 8; ++j) { int d = d0 + 8 * (j >> 2) + (j & 3); v[j] = d < 28 ? src[d * 448] : 0u; }
            ((u32x4*)(ws + WS_WO))[item] = u32x4{v[0] | (v[1] << 16), v[2] | (v[3] << 16), v[4] | (v[5] << 16), v[6] | (v[7] << 16)};
        }
        BAR();
        LAS cf* f = fb + w * 7 * RSTR;
        cf a[8];
#pragma unroll
        for (int p = 0; p < 7; ++p) a[p] = f[p * RSTR + lane];
        dft7<-1>(a);
#pragma unroll
        for (int s = 1; s < 7; ++s) a[s] = cmul<-1>(a[s], tw(lane * s, 1.f / 448));
#pragma unroll
        for (int s = 0; s < 7; ++s) f[s * RSTR + lane] = a[s];
        BAR();
        const int l2 = lane < 56 ? lane : 55;
        {
            const int s = l2 >> 3, r0 = l2 & 7;
#pragma unroll
            for (int i = 0; i < 8; ++i) a[i] = f[s * RSTR + 8 * i + r0];
            dft8<-1>(a);
#pragma unroll
            for (int i = 1; i < 8; ++i) a[i] = cmul<-1>(a[i], tw(r0 * i, 1.f / 64));
            if (lane < 56) {
#pragma unroll
                for (int i = 0; i < 8; ++i) f[s * RSTR + 8 * i + r0] = a[i];
            }
        }
        WAITL();
        {
            const int m1 = l2 / 7, s = l2 - 7 * m1;
#pragma unroll
            for (int i = 0; i < 8; ++i) a[i] = f[s * RSTR + 8 * m1 + i];
            dft8<-1>(a);
        }
        cf* y = (cf*)(ws + WS_Y1) + (size_t)b * NQ;
        if (w != 0) {
            if (lane < 56) {
#pragma unroll
                for (int i = 0; i < 4; ++i) y[w * 225 + lane + 56 * i] = a[i];
#pragma unroll
                for (int i = 4; i < 8; ++i) y[(8 - w) * 225 + 448 - lane - 56 * i] = cconj(a[i]);
                if (lane == 0) { y[w * 225 + 224] = a[4]; y[(8 - w) * 225] = cconj(a[0]); }
            }
        } else {
            WAITL();
            if (lane < 56) {
#pragma unroll
                for (int i = 0; i < 8; ++i) f[lane + 56 * i] = a[i];
            }
            if (lane == 0) f[448] = a[0];
            WAITL();
            const cf w4 = tw(b + 128, 1.f / 512) * 0.5f;
#pragma unroll
            for (int i = 0; i < 4; ++i) {
                const int k = lane + 64 * i;
                if (k <= 224) {
                    cf u = f[k], v = cconj(f[448 - k]);
                    y[k] = (u + v) * 0.5f;
                    y[4 * 225 + k] = cmul<-1>(u - v, w4);
                }
            }
        }
    }
}

DEVI void mid_fft(int g, int w, int lane, LAS cf* mb, const cf* __restrict__ y1, cf* __restrict__ y2,
                  const unsigned short* __restrict__ wr_, const unsigned short* __restrict__ wi_, bool active) {
    const int col = lane & 7, q = g * 8 + col, c = q / 225, k2 = q - c * 225, cs = c > 4 ? c - 8 : c;
    const int jj = lane >> 3, j = 8 * w + jj;
    const int db = lane >> 5, j0 = (lane >> 3) & 3, d0 = 2 * w + db;
    cf a[8]; cf fl[8];
    if (active) {
#pragma unroll
        for (int i = 0; i < 8; ++i) a[i] = y1[(size_t)(32 * i + j) * NQ + q];
#pragma unroll
        for (int i = 0; i < 8; ++i) {
            const int d = d0 + 8 * (j0 + 4 * (i >> 2)) + 64 * (i & 3), k1 = (cs + 8 * d) & 2047;
            fl[i] = cf{bflo(wi_[k1 * 225 + k2]), bflo(wr_[k1 * 225 + k2])};
        }
        dft8<-1>(a);
#pragma unroll
        for (int i = 1; i < 8; ++i) a[i] = cmul<-1>(a[i], tw(j * i, 1.f / 256));
#pragma unroll
        for (int i = 0; i < 8; ++i) mb[(i * 32 + j) * 8 + col] = a[i];
    }
    BAR();
    if (active) {
#pragma unroll
        for (int i = 0; i < 8; ++i) a[i] = mb[(d0 * 32 + 4 * i + j0) * 8 + col];
        dft8<-1>(a);
#pragma unroll
        for (int i = 1; i < 8; ++i) a[i] = cmul<-1>(a[i], tw(j0 * i, 1.f / 32));
#pragma unroll
        for (int i = 0; i < 8; ++i) mb[(d0 * 32 + 4 * i + j0) * 8 + col] = a[i];
        WAITL();
#pragma unroll
        for (int i = 0; i < 8; ++i) a[i] = mb[(d0 * 32 + 4 * (j0 + 4 * (i >> 2)) + (i & 3)) * 8 + col];
        dft4<-1>(a[0], a[1], a[2], a[3]); dft4<-1>(a[4], a[5], a[6], a[7]);
#pragma unroll
        for (int i = 0; i < 8; ++i) a[i] = cmul<1>(a[i], fl[i]);
        dft4<1>(a[0], a[1], a[2], a[3]); dft4<1>(a[4], a[5], a[6], a[7]);
#pragma unroll
        for (int i = 0; i < 8; ++i) mb[(d0 * 32 + 4 * (j0 + 4 * (i >> 2)) + (i & 3)) * 8 + col] = a[i];
        WAITL();
#pragma unroll
        for (int i = 0; i < 8; ++i) a[i] = mb[(d0 * 32 + 4 * i + j0) * 8 + col];
#pragma unroll
        for (int i = 1; i < 8; ++i) a[i] = cmul<1>(a[i], tw(j0 * i, 1.f / 32));
        dft8<1>(a);
#pragma unroll
        for (int i = 0; i < 8; ++i) mb[(d0 * 32 + 4 * i + j0) * 8 + col] = a[i];
    }
    BAR();
    if (active) {
#pragma unroll
        for (int i = 0; i < 8; ++i) a[i] = mb[(i * 32 + j) * 8 + col];
#pragma unroll
        for (int i = 1; i < 8; ++i) a[i] = cmul<1>(a[i], tw(j * i, 1.f / 256));
        dft8<1>(a);
#pragma unroll
        for (int i = 0; i < 8; ++i) y2[(size_t)(32 * i + j) * NQ + q] = a[i];
    }
}

constexpr int K2_KV = 0, K2_M = 131072, K2_LDS = K2_M + 16384 + 64;

DEVI float xhalf_max(float m) {
    auto rr = __builtin_amdgcn_permlane32_swap(__float_as_uint(m), __float_as_uint(m), false, false);
    return __builtin_fmaxf(__uint_as_float(rr[0]), __uint_as_float(rr[1]));
}
DEVI float max16(const f32x16& s) {
    float m = __builtin_fmaxf(s[0], s[1]);
#pragma unroll
    for (int i = 2; i < 16; ++i) m = __builtin_fmaxf(m, s[i]);
    return m;
}
DEVI bf16x8 set_ref(bf16x8 q1, float m, int h) {
    const float nm = -m;
    const unsigned b0 = __float_as_uint(nm) & 0xffff0000u; const float r1 = nm - __uint_as_float(b0);
    const unsigned b1 = __float_as_uint(r1) & 0xffff0000u; const float r2 = r1 - __uint_as_float(b1);
    const unsigned b2 = __float_as_uint(r2) & 0xffff0000u;
    u32x4 u = __builtin_bit_cast(u32x4, q1);
    if (h) { u[2] = (b0 >> 16) | b1; u[3] = b2 >> 16; }
    return __builtin_bit_cast(bf16x8, u);
}
DEVI void attn_tile(bf16x8 k0, bf16x8 k1, bf16x8 v0, bf16x8 v1, bf16x8 q0, bf16x8 q1, f32x16& o) {
    f32x16 s = MFMA32(k0, q0, f32x16{});
    s = MFMA32(k1, q1, s);
    u32x4 p0, p1;
#pragma unroll
    for (int e = 0; e < 4; ++e) {
        p0[e] = cvtpk(__builtin_amdgcn_exp2f(s[2 * e]), __builtin_amdgcn_exp2f(s[2 * e + 1]));
        p1[e] = cvtpk(__builtin_amdgcn_exp2f(s[8 + 2 * e]), __builtin_amdgcn_exp2f(s[9 + 2 * e]));
    }
    o = MFMA32(v0, __builtin_bit_cast(bf16x8, p0), o);
    o = MFMA32(v1, __builtin_bit_cast(bf16x8, p1), o);
}

__global__ void __launch_bounds__(512, 2) k2_kernel(const unsigned short* __restrict__ fwr, const unsigned short* __restrict__ fwi,
                                                    char* __restrict__ ws) {
    extern __shared__ __attribute__((aligned(16))) char lds[];
    const int tid = threadIdx.x, lane = tid & 63, wid = __builtin_amdgcn_readfirstlane(tid >> 6);
    const int bid = blockIdx.x;
    const unsigned lds0 = (unsigned)(uintptr_t)lds;
    const int head = (bid & 7) * 2 + ((bid >> 3) & 1), ti = bid >> 4;
    const int qg = wid & 3, sp = wid >> 2, h = lane >> 5;
    const char* kvsrc = ws + WS_KV + (size_t)head * 262144 + lane * 16;
    auto ldchunk = [&](int c) {
#pragma unroll
        for (int i = 0; i < 8; ++i) glds16(kvsrc + c * 65536 + (i * 8 + wid) * 1024, lds0 + K2_KV + (c & 1) * 65536 + (i * 8 + wid) * 1024);
    };
    const u32x4* qsrc = (const u32x4*)(ws + WS_QF) + ((head * 64 + ti * 4 + qg) * 2) * 64 + lane;
    bf16x8 q0 = __builtin_bit_cast(bf16x8, qsrc[0]), q1 = __builtin_bit_cast(bf16x8, qsrc[64]);
    ldchunk(0); ldchunk(1);
    mid_fft(bid, wid & 3, lane, (LAS cf*)(lds + K2_M), (const cf*)(ws + WS_Y1), (cf*)(ws + WS_Y2), fwr, fwi, wid >= 4 && bid < 225);
    WAITV(0); BAR();
    const LAS bf16x8* kv = (const LAS bf16x8*)(lds + K2_KV) + lane;
    {
        f32x16 s = MFMA32(kv[0], q0, f32x16{});
        s = MFMA32(kv[64], q1, s);
        q1 = set_ref(q1, xhalf_max(max16(s)), h);
    }
    f32x16 o = f32x16{};
#pragma unroll 1
    for (int c = 0; c < 4; ++c) {
        const LAS bf16x8* p = kv + (c & 1) * 4096 + sp * 2048;
#pragma unroll 2
        for (int t = 0; t < 8; ++t) attn_tile(p[t * 256], p[t * 256 + 64], p[t * 256 + 128], p[t * 256 + 192], q0, q1, o);
        if (c < 3) { WAITV(0); BAR(); if (c < 2) ldchunk(c + 2); }
    }
    LAS f32x4* cb = (LAS f32x4*)(lds + K2_M) + qg * 256 + lane;
    LAS unsigned* flag = (LAS unsigned*)(lds + K2_M + 16384);
    if (tid == 0) *flag = 0u;
    if (sp) {
#pragma unroll
        for (int e = 0; e < 4; ++e) cb[e * 64] = f32x4{o[4 * e], o[4 * e + 1], o[4 * e + 2], o[4 * e + 3]};
    }
    BAR();
    if (!sp) {
#pragma unroll
        for (int e = 0; e < 4; ++e) { f32x4 v = cb[e * 64]; o[4 * e] += v[0]; o[4 * e + 1] += v[1]; o[4 * e + 2] += v[2]; o[4 * e + 3] += v[3]; }
        float l = __shfl(o[12], (lane & 31) + 32);
        float chk = 0.f;
#pragma unroll
        for (int e = 0; e < 16; ++e) chk += o[e] * 0.f;
        const bool bad = !(l > 0.25f && l < 1.0e18f && chk == 0.f);
        if (__any(bad)) {
            const u32x4* g = (const u32x4*)(ws + WS_KV) + (size_t)head * 16384 + lane;
            q1 = set_ref(q1, 0.f, h);
            float m = -INFINITY;
#pragma unroll 1
            for (int t = 0; t < 64; ++t) {
                f32x16 s = MFMA32(__builtin_bit_cast(bf16x8, g[t * 256]), q0, f32x16{});
                s = MFMA32(__builtin_bit_cast(bf16x8, g[t * 256 + 64]), q1, s);
                m = __builtin_fmaxf(m, max16(s));
            }
            q1 = set_ref(q1, xhalf_max(m), h);
            o = f32x16{};
#pragma unroll 1
            for (int t = 0; t < 64; ++t)
                attn_tile(__builtin_bit_cast(bf16x8, g[t * 256]), __builtin_bit_cast(bf16x8, g[t * 256 + 64]),
                          __builtin_bit_cast(bf16x8, g[t * 256 + 128]), __builtin_bit_cast(bf16x8, g[t * 256 + 192]), q0, q1, o);
            l = __shfl(o[12], (lane & 31) + 32);
        }
        const float inv = 1.0f / l;
        u32x4 f0, f1;
#pragma unroll
        for (int e = 0; e < 4; ++e) { f0[e] = cvtpk(o[2 * e] * inv, o[2 * e + 1] * inv); f1[e] = cvtpk(o[8 + 2 * e] * inv, o[9 + 2 * e] * inv); }
        u32x4* dst = (u32x4*)(ws + WS_OF) + (((ti * 4 + qg) * 16 + head) * 2) * 64 + lane;
        dst[0] = f0; dst[64] = f1;
    }
}

constexpr int K3_O = 0, K3_F = 32768, K3_LDS = K3_F + 4 * RFB;

__global__ void __launch_bounds__(512, 2) k3_kernel(unsigned short* __restrict__ out, char* __restrict__ ws) {
    extern __shared__ __attribute__((aligned(16))) char lds[];
    const int tid = threadIdx.x, lane = tid & 63, wid = __builtin_amdgcn_readfirstlane(tid >> 6);
    const int bid = blockIdx.x;
    const int tile = bid >> 2, cq = bid & 3;
    const u32x4* osrc = (const u32x4*)(ws + WS_OF) + tile * 2048 + tid;
    u32x4 ov[4];
#pragma unroll
    for (int i = 0; i < 4; ++i) ov[i] = osrc[i * 512];
    bf16x8 wf[16];
    const int cb = cq * 7 + (wid < 7 ? wid : 6);
#pragma unroll
    for (int i = 0; i < 16; ++i) wf[i] = __builtin_bit_cast(bf16x8, ((const u32x4*)(ws + WS_WO))[(cb * 16 + i) * 64 + lane]);
    const int b = bid, w = wid & 3;
    LAS cf* fb = (LAS cf*)(lds + K3_F);
    LAS cf* f = fb + w * 7 * RSTR;
    if (wid >= 4) {
        const cf* y = (const cf*)(ws + WS_Y2) + (size_t)b * NQ;
        const int l2 = lane < 56 ? lane : 55;
        cf a[8];
        if (w != 0) {
#pragma unroll
            for (int i = 0; i < 4; ++i) a[i] = y[w * 225 + l2 + 56 * i];
#pragma unroll
            for (int i = 4; i < 8; ++i) a[i] = cconj(y[(8 - w) * 225 + 448 - l2 - 56 * i]);
            if (lane == 0) { a[0] = (a[0] + cconj(y[(8 - w) * 225])) * 0.5f; a[4] = (a[4] + y[w * 225 + 224]) * 0.5f; }
        } else {
            const cf w4 = tw(b, 1.f / 512);
#pragma unroll
            for (int i = 0; i < 8; ++i) {
                const int k = l2 + 56 * i, kk = i < 4 ? k : 448 - k;
                cf z0 = y[kk], z4 = cmul<1>(y[4 * 225 + kk], w4);
                if (i >= 4) { z0 = cconj(z0); z4 = cconj(z4); }
                if ((i & 3) == 0 && lane == 0) { z0.y = 0.f; z4.y = 0.f; }
                a[i] = addrot<1>(z0, z4);
            }
        }
        dft8<1>(a);
        {
            const int m1 = l2 / 7, s = l2 - 7 * m1;
            if (lane < 56) {
#pragma unroll
                for (int i = 0; i < 8; ++i) f[s * RSTR + 8 * m1 + i] = a[i];
            }
        }
        WAITL();
        {
            const int s = l2 >> 3, r0 = l2 & 7;
#pragma unroll
            for (int i = 0; i < 8; ++i) a[i] = f[s * RSTR + 8 * i + r0];
#pragma unroll
            for (int i = 1; i < 8; ++i) a[i] = cmul<1>(a[i], tw(r0 * i, 1.f / 64));
            dft8<1>(a);
            if (lane < 56) {
#pragma unroll
                for (int i = 0; i < 8; ++i) f[s * RSTR + 8 * i + r0] = a[i];
            }
        }
        WAITL();
#pragma unroll
        for (int s = 0; s < 7; ++s) a[s] = f[s * RSTR + lane];
#pragma unroll
        for (int s = 1; s < 7; ++s) a[s] = cmul<1>(a[s], tw(lane * s, 1.f / 448));
        dft7<1>(a);
#pragma unroll
        for (int p = 0; p < 7; ++p) f[p * RSTR + lane] = a[p];
    }
#pragma unroll
    for (int i = 0; i < 4; ++i) ((LAS u32x4*)(lds + K3_O))[i * 512 + tid] = ov[i];
    BAR();
    if (wid >= 4 && tid - 256 < 224) {
        const int t = tid - 256, n2 = 2 * t, pos = (n2 >> 6) * RSTR + (n2 & 63);
        const float sc = 1.0f / (2048.0f * 448.0f), sc2 = 2.f * sc;
        f32x4 u0 = *(LAS f32x4*)(fb + pos), u1 = *(LAS f32x4*)(fb + 7 * RSTR + pos), u2 = *(LAS f32x4*)(fb + 14 * RSTR + pos), u3 = *(LAS f32x4*)(fb + 21 * RSTR + pos);
        cf w1 = tw(b, 1.f / 2048) * sc2, w2 = tw(2 * b, 1.f / 2048) * sc2, w3 = tw(3 * b, 1.f / 2048) * sc2;
        cf z0 = cf{u0[0], u0[2]} * sc, z4 = cf{u0[1], u0[3]} * sc;
        cf a0 = cmul<1>(cf{u1[0], u1[1]}, w1), a1 = cmul<1>(cf{u1[2], u1[3]}, w1);
        cf z1r = cf{a0.x, a1.x}, z1i = cf{a0.y, a1.y};
        a0 = cmul<1>(cf{u2[0], u2[1]}, w2); a1 = cmul<1>(cf{u2[2], u2[3]}, w2);
        cf z2r = cf{a0.x, a1.x}, z2i = cf{a0.y, a1.y};
        a0 = cmul<1>(cf{u3[0], u3[1]}, w3); a1 = cmul<1>(cf{u3[2], u3[3]}, w3);
        cf z3r = cf{a0.x, a1.x}, z3i = cf{a0.y, a1.y};
        const float rr = 0.70710678118654752f;
        cf e0 = z0 + z4, e1 = z0 - z4;
        cf p0 = e0 + z2r, p2 = e0 - z2r, p1 = e1 - z2i, p3 = e1 + z2i;
        cf m0 = z1r + z3r, m2 = z3i - z1i;
        cf qa = (z1r - z3r) * rr, qb = (z1i + z3i) * rr;
        cf m1 = qa - qb, m3 = -qa - qb;
        cf yv[8] = {p0 + m0, p1 + m1, p2 + m2, p3 + m3, p0 - m0, p1 - m1, p2 - m2, p3 - m3};
#pragma unroll
        for (int a = 0; a < 8; ++a) *(unsigned*)(out + (size_t)(256 * a + b) * 896 + 448 + n2) = cvtpk(yv[a].x, yv[a].y);
    }
    if (wid < 7) {
        const int g = lane >> 4;
        const LAS bf16x8* op = (const LAS bf16x8*)(lds + K3_O) + (g >> 1) * 64 + (g & 1) * 32 + (lane & 15);
        f32x4 c0 = f32x4{}, c1 = f32x4{};
#pragma unroll
        for (int i = 0; i < 16; ++i) { c0 = MFMA16(wf[i], op[i * 128], c0); c1 = MFMA16(wf[i], op[i * 128 + 16], c1); }
        unsigned short* dst = out + (size_t)(tile * 32 + (lane & 15)) * 896 + cb * 16 + g * 4;
        *(u32x2*)dst = u32x2{cvtpk(c0[0], c0[1]), cvtpk(c0[2], c0[3])};
        *(u32x2*)(dst + 16 * 896) = u32x2{cvtpk(c1[0], c1[1]), cvtpk(c1[2], c1[3])};
    }
}

extern "C" void kernel_launch(void* const* d_in, const int* in_sizes, int n_in, void* d_out, int out_size, void* d_ws,
                              size_t ws_size, hipStream_t stream) {
    const unsigned short* ax = (const unsigned short*)d_in[0];
    const unsigned short* wqkv = (const unsigned short*)d_in[1];
    const unsigned short* wo = (const unsigned short*)d_in[2];
    const unsigned short* bx = (const unsigned short*)d_in[3];
    const unsigned short* fwr = (const unsigned short*)d_in[4];
    const unsigned short* fwi = (const unsigned short*)d_in[5];
    char* ws = (char*)d_ws;
    (void)hipFuncSetAttribute((const void*)k1_kernel, hipFuncAttributeMaxDynamicSharedMemorySize, K1_LDS);
    (void)hipFuncSetAttribute((const void*)k2_kernel, hipFuncAttributeMaxDynamicSharedMemorySize, K2_LDS);
    (void)hipFuncSetAttribute((const void*)k3_kernel, hipFuncAttributeMaxDynamicSharedMemorySize, K3_LDS);
    k1_kernel<<<256, 512, K1_LDS, stream>>>(ax, wqkv, wo, bx, ws);
    k2_kernel<<<256, 512, K2_LDS, stream>>>(fwr, fwi, ws);
    k3_kernel<<<256, 512, K3_LDS, stream>>>((unsigned short*)d_out, ws);
}
```

```cpp
#include <hip/hip_runtime.h>
#include <hip/hip_bf16.h>
#include <stdint.h>

typedef __attribute__((ext_vector_type(8))) short bf16x8;
typedef __attribute__((ext_vector_type(4))) short s16x4;
typedef __attribute__((ext_vector_type(16))) float f32x16;
typedef __attribute__((ext_vector_type(4))) float f32x4;
typedef __attribute__((ext_vector_type(4))) unsigned u32x4;
typedef __attribute__((ext_vector_type(2))) unsigned u32x2;
typedef __attribute__((ext_vector_type(2))) float cf;
#define LAS __attribute__((address_space(3)))
#define DEVI __device__ __forceinline__
#define MFMA32(a, b, c) __builtin_amdgcn_mfma_f32_32x32x16_bf16(a, b, c, 0, 0, 0)
#define MFMA16(a, b, c) __builtin_amdgcn_mfma_f32_16x16x32_bf16(a, b, c, 0, 0, 0)

constexpr int NQ = 1800;
constexpr int F_GEN = 0, F_KV = 256, F_O = F_KV + 256 * 128, F_Y1 = F_O + 256 * 128, FREP = 1024 + 256, F_Y2 = F_Y1 + 8 * FREP;
constexpr int WS_KV = 131072;
constexpr int WS_WO = WS_KV + 16 * 64 * 4 * 1024;
constexpr int WS_OF = WS_WO + 28 * 16 * 1024;
constexpr int WS_Y1 = WS_OF + 64 * 16 * 2 * 1024;
constexpr int WS_Y2 = WS_Y1;
constexpr int WS_END = WS_Y1 + 256 * NQ * 8;
constexpr float QSCALE = 0.18898223650461362f * 1.4426950408889634f;

typedef __attribute__((ext_vector_type(2))) __bf16 bf16x2_t;
DEVI unsigned cvtpk(float lo, float hi) { return __builtin_bit_cast(unsigned, __builtin_convertvector(cf{lo, hi}, bf16x2_t)); }
DEVI float bflo(unsigned u) { return __uint_as_float(u << 16); }
DEVI float bfhi(unsigned u) { return __uint_as_float(u & 0xffff0000u); }
DEVI void glds16(const void* g, unsigned lds_base) {
    unsigned sv;
    asm volatile("s_mov_b32 %0, m0\n\ts_mov_b32 m0, %2\n\ts_nop 0\n\tglobal_load_lds_dwordx4 %1, off\n\ts_mov_b32 m0, %0"
                 : "=&s"(sv) : "v"(g), "s"(__builtin_amdgcn_readfirstlane(lds_base)) : "memory");
}
#define WAITV(N) asm volatile("s_waitcnt vmcnt(" #N ")" ::: "memory")
#define WAITL() asm volatile("s_waitcnt lgkmcnt(0)" ::: "memory")
#define BAR() asm volatile("s_waitcnt lgkmcnt(0)\n\ts_barrier" ::: "memory")

#define STAMP(K, I)
#define STAMP_DECL
#define STAMP_FLUSH(K)
template <int DIR> DEVI cf cmul(cf a, cf w) {
    cf t, r;
    asm("v_pk_mul_f32 %0, %1, %2 op_sel:[1,0] op_sel_hi:[0,0]" : "=v"(t) : "v"(a), "v"(w));
    if (DIR > 0) asm("v_pk_fma_f32 %0, %1, %2, %3 op_sel:[1,0,0] op_sel_hi:[1,1,1] neg_lo:[0,0,1]" : "=v"(r) : "v"(w), "v"(a), "v"(t));
    else asm("v_pk_fma_f32 %0, %1, %2, %3 op_sel:[1,0,0] op_sel_hi:[1,1,1] neg_hi:[0,0,1]" : "=v"(r) : "v"(w), "v"(a), "v"(t));
    return r;
}
template <int DIR> DEVI cf addrot(cf t, cf x) {
    cf r;
    if (DIR > 0) asm("v_pk_add_f32 %0, %1, %2 op_sel:[1,0] op_sel_hi:[0,1] neg_lo:[1,0]" : "=v"(r) : "v"(x), "v"(t));
    else asm("v_pk_add_f32 %0, %1, %2 op_sel:[1,0] op_sel_hi:[0,1] neg_hi:[1,0]" : "=v"(r) : "v"(x), "v"(t));
    return r;
}
DEVI cf cconj(cf a) { return cf{a.x, -a.y}; }
DEVI cf tw(int num, float inv_den) {
    const float x = (float)num * inv_den;
    cf w = cf{__builtin_amdgcn_sinf(x), __builtin_amdgcn_cosf(x)};
    asm("s_nop 0" : "+v"(w));
    return w;
}
template <int DIR> DEVI void dft4(cf& a0, cf& a1, cf& a2, cf& a3) {
    cf t0 = a0 + a2, t1 = a0 - a2, t2 = a1 + a3, t3 = a1 - a3;
    a0 = t0 + t2; a2 = t0 - t2; a1 = addrot<DIR>(t1, t3); a3 = addrot<-DIR>(t1, t3);
}
template <int DIR> DEVI void dft8(cf* a) {
    cf e0 = a[0], e1 = a[2], e2 = a[4], e3 = a[6], o0 = a[1], o1 = a[3], o2 = a[5], o3 = a[7];
    dft4<DIR>(e0, e1, e2, e3); dft4<DIR>(o0, o1, o2, o3);
    const float r = 0.70710678118654752f;
    o1 = addrot<DIR>(o1, o1); o3 = addrot<-DIR>(o3, o3);
    a[0] = e0 + o0; a[4] = e0 - o0; a[1] = e1 + o1 * r; a[5] = e1 - o1 * r;
    a[2] = addrot<DIR>(e2, o2); a[6] = addrot<-DIR>(e2, o2); a[3] = e3 - o3 * r; a[7] = e3 + o3 * r;
}
template <int DIR> DEVI void dft7(cf* a) {
    const float c1 = 0.62348980185873353f, c2 = -0.22252093395631440f, c3 = -0.90096886790241913f;
    const float s1 = 0.78183148246802981f, s2 = 0.97492791218182361f, s3 = 0.43388373911755812f;
    cf t1 = a[1] + a[6], t2 = a[2] + a[5], t3 = a[3] + a[4], u1 = a[1] - a[6], u2 = a[2] - a[5], u3 = a[3] - a[4];
    cf A1 = a[0] + t1 * c1 + t2 * c2 + t3 * c3, A2 = a[0] + t1 * c2 + t2 * c3 + t3 * c1, A3 = a[0] + t1 * c3 + t2 * c1 + t3 * c2;
    cf B1 = u1 * s1 + u2 * s2 + u3 * s3, B2 = u1 * s2 - u2 * s3 - u3 * s1, B3 = u1 * s3 - u2 * s1 + u3 * s2;
    a[0] = a[0] + t1 + t2 + t3;
    a[1] = addrot<DIR>(A1, B1); a[6] = addrot<-DIR>(A1, B1); a[2] = addrot<DIR>(A2, B2); a[5] = addrot<-DIR>(A2, B2);
    a[3] = addrot<DIR>(A3, B3); a[4] = addrot<-DIR>(A3, B3);
}
constexpr int RSTR = 72;
constexpr int RFB = 7 * RSTR * 8;

#define CSTAMP0
#define CSTAMP1
typedef __amdgpu_buffer_rsrc_t rsrc_t;
DEVI void st128(rsrc_t r, int off, u32x4 v) { __builtin_amdgcn_raw_buffer_store_b128(v, r, off, 0, 16); }
DEVI void st64(rsrc_t r, int off, cf v) { __builtin_amdgcn_raw_buffer_store_b64(__builtin_bit_cast(u32x2, v), r, off, 0, 16); }
DEVI void st32(rsrc_t r, int off, unsigned v) { __builtin_amdgcn_raw_buffer_store_b32(v, r, off, 0, 16); }
DEVI u32x4 ld128(rsrc_t r, int off) { return __builtin_amdgcn_raw_buffer_load_b128(r, off, 0, 16); }
DEVI cf ld64(rsrc_t r, int off) { return __builtin_bit_cast(cf, __builtin_amdgcn_raw_buffer_load_b64(r, off, 0, 16)); }
DEVI unsigned ld32(rsrc_t r, int off) { return __builtin_amdgcn_raw_buffer_load_b32(r, off, 0, 16); }
DEVI void glds16c(const void* g, unsigned lds_base) {
    unsigned sv;
    asm volatile("s_mov_b32 %0, m0\n\ts_mov_b32 m0, %2\n\ts_nop 0\n\tglobal_load_lds_dwordx4 %1, off sc1\n\ts_mov_b32 m0, %0"
                 : "=&s"(sv) : "v"(g), "s"(__builtin_amdgcn_readfirstlane(lds_base)) : "memory");
}
#define FENCE() asm volatile("" ::: "memory")
constexpr int SPIN_CAP = 1 << 18;
DEVI void arrive(LAS unsigned* c, int) {
    asm volatile("s_waitcnt lgkmcnt(0)\n\ts_mov_b64 exec, 1\n\tds_add_u32 %0, %1\n\ts_mov_b64 exec, -1" :: "v"(c), "v"(1u) : "memory");
}
DEVI unsigned arrive_rtn(LAS unsigned* c) {
    unsigned v, r;
    asm volatile("s_waitcnt lgkmcnt(0)\n\ts_mov_b64 exec, 1\n\tds_add_rtn_u32 %0, %2, %3\n\ts_waitcnt lgkmcnt(0)\n\tv_readfirstlane_b32 %1, %0\n\ts_mov_b64 exec, -1"
                 : "=&v"(v), "=s"(r) : "v"(c), "v"(1u) : "memory");
    return r;
}
DEVI void waitc(LAS unsigned* c, unsigned n) {
    unsigned v, sv, it;
    asm volatile("s_mov_b32 %2, 0x200000\n"
                 "1:\n\tds_read_b32 %0, %3\n\ts_waitcnt lgkmcnt(0)\n\tv_readfirstlane_b32 %1, %0\n\ts_cmp_ge_u32 %1, %4\n\ts_cbranch_scc1 2f\n\t"
                 "s_sleep 2\n\ts_sub_u32 %2, %2, 1\n\ts_cbranch_scc0 1b\n2:"
                 : "=&v"(v), "=&s"(sv), "=&s"(it) : "v"(c), "s"(n) : "memory", "scc");
}
DEVI void publish(rsrc_t r, LAS unsigned* c, int flag_off, unsigned tag, int lane, int nrep) {
    WAITV(0);
    if (arrive_rtn(c) == 3u && lane < nrep) st32(r, flag_off + lane * FREP, tag);
}
DEVI void poll32(rsrc_t r, int off, unsigned tag) {
#pragma unroll 1
    for (int i = 0; i < SPIN_CAP; ++i) { if (__all(ld32(r, off) == tag)) break; __builtin_amdgcn_s_sleep(2); FENCE(); }
    FENCE();
}
DEVI void poll128(rsrc_t r, int off, unsigned tag) {
#pragma unroll 1
    for (int i = 0; i < SPIN_CAP; ++i) {
        const u32x4 v = ld128(r, off);
        if (__all(v[0] == tag && v[1] == tag && v[2] == tag && v[3] == tag)) break;
        __builtin_amdgcn_s_sleep(10); FENCE();
    }
    FENCE();
}

DEVI float xhalf_max(float m) {
    auto rr = __builtin_amdgcn_permlane32_swap(__float_as_uint(m), __float_as_uint(m), false, false);
    return __builtin_fmaxf(__uint_as_float(rr[0]), __uint_as_float(rr[1]));
}
DEVI float max16(const f32x16& s) {
    float m = __builtin_fmaxf(s[0], s[1]);
#pragma unroll
    for (int i = 2; i < 16; ++i) m = __builtin_fmaxf(m, s[i]);
    return m;
}
DEVI bf16x8 set_ref(bf16x8 q1, float m, int h) {
    const float nm = -m;
    const unsigned b0 = __float_as_uint(nm) & 0xffff0000u; const float r1 = nm - __uint_as_float(b0);
    const unsigned b1 = __float_as_uint(r1) & 0xffff0000u; const float r2 = r1 - __uint_as_float(b1);
    const unsigned b2 = __float_as_uint(r2) & 0xffff0000u;
    u32x4 u = __builtin_bit_cast(u32x4, q1);
    if (h) { u[2] = (b0 >> 16) | b1; u[3] = b2 >> 16; }
    return __builtin_bit_cast(bf16x8, u);
}
DEVI bf16x8 trfrag(const LAS char* p, int rowstride) {
    s16x4 lo = __builtin_amdgcn_ds_read_tr16_b64_v4i16((LAS s16x4*)p);
    s16x4 hi = __builtin_amdgcn_ds_read_tr16_b64_v4i16((LAS s16x4*)(p + 4 * rowstride));
    return __builtin_shufflevector(lo, hi, 0, 1, 2, 3, 4, 5, 6, 7);
}
#define SB() __builtin_amdgcn_sched_barrier(0)
#define EX(x) __builtin_amdgcn_exp2f(x)
#define BC8(x) __builtin_bit_cast(bf16x8, x)

template <bool V> struct BoolC { static constexpr bool value = V; };
constexpr int L_WP = 0, L_XS = 50240, L_F = L_XS + 4 * 20480, L_YA = L_F + 16384, L_C = L_YA + 14400, L_END = L_C + 256;
enum { C_W = 0, C_Q, C_PK, C_PO, C_PY, C_WQ, C_KV, C_T0, C_T1, C_R0, C_R1, C_R2, C_R3, C_D0, C_D1, C_D2, C_O, C_OS, C_QS, C_OX, C_OY, C_B0, C_B1, C_B2, C_B3, C_B4, C_B5, C_B6, C_M = 40 };
constexpr int NT0 = 16;

__global__ void __launch_bounds__(512, 2) fused_kernel(const unsigned short* __restrict__ x, const unsigned short* __restrict__ wqkv,
                                                       const unsigned short* __restrict__ wo, const unsigned short* __restrict__ bx,
                                                       const unsigned short* __restrict__ fwr, const unsigned short* __restrict__ fwi,
                                                       unsigned short* __restrict__ out, char* ws) {
    extern __shared__ __attribute__((aligned(16))) char lds[];
    const int tid = threadIdx.x, lane = tid & 63, wid = __builtin_amdgcn_readfirstlane(tid >> 6);
    const int bid = blockIdx.x, xcd = bid & 7, isv = (bid >> 3) & 1, ti = bid >> 4, head = 2 * xcd + isv;
    const unsigned lds0 = (unsigned)(uintptr_t)lds;
    const rsrc_t rs = __builtin_amdgcn_make_buffer_rsrc((void*)ws, (short)0, WS_END, 0x00020000);
    LAS unsigned* cnt = (LAS unsigned*)(lds + L_C);
    STAMP_DECL STAMP(0, 0);
    unsigned genv;
    asm volatile("global_load_dword %0, %1, %2 sc1" : "=v"(genv) : "v"(0), "s"(ws) : "memory");
    if (tid < 64) cnt[tid] = 0u;
    const char* kvsrc = ws + WS_KV + head * 262144 + lane * 16;
    const int otile = xcd * 8 + (bid >> 5), cq = (bid >> 3) & 3;
    const int w = wid & 3;
    const int r = lane & 31, h = lane >> 5, sp = wid >> 2;
    const LAS bf16x8* kv = (const LAS bf16x8*)lds + lane;
    LAS bf16x8* qs_ = (LAS bf16x8*)(lds + L_YA) + w * 128 + lane;
    f32x16 sa, sb, o = f32x16{};
    bf16x8 q0, q1, k0, k1, v0, v1;
    u32x4 p0, p1 = u32x4{};
    float e0 = 0.f, e1 = 0.f, e2 = 0.f, e3 = 0.f;
    unsigned tag;
    cf t448[7], t64[8], tb1, tb2, tb3, tb4;
#define E4(SA, i) e0 = EX(SA[i]); e1 = EX(SA[i + 1]); e2 = EX(SA[i + 2]); e3 = EX(SA[i + 3]); SB();
#define STEP(SA, SN, PV, PK) \
        SN = MFMA32(k0, q0, f32x16{}); SB(); k0 = (PK)[0]; v0 = (PV)[128]; p1[2] = cvtpk(e0, e1); p1[3] = cvtpk(e2, e3); SB(); \
        E4(SA, 0) \
        o = MFMA32(v1, BC8(p1), o); p0[0] = cvtpk(e0, e1); p0[1] = cvtpk(e2, e3); SB(); \
        E4(SA, 4) \
        SN = MFMA32(k1, q1, SN); SB(); k1 = (PK)[64]; v1 = (PV)[192]; p0[2] = cvtpk(e0, e1); p0[3] = cvtpk(e2, e3); SB(); \
        E4(SA, 8) \
        o = MFMA32(v0, BC8(p0), o); p1[0] = cvtpk(e0, e1); p1[1] = cvtpk(e2, e3); SB(); \
        E4(SA, 12)
    if (wid < 4) {
        const char* wsrc = (const char*)wqkv + 896 * (1 + isv) + 112 * xcd;
        const char* xsrc = (const char*)x + (size_t)(ti * 128 + w * 32 + (lane >> 3)) * 896;
        {
            const int c = w * 64 + lane, row = c / 7;
            int ro = row, co = c - row * 7;
#pragma unroll
            for (int i = 0; i < 13; ++i) {
                if (i < 12 || w == 0) glds16(wsrc + ro * 2688 + co * 16, lds0 + L_WP + (w + 4 * i) * 1024);
                co += 4; ro += 36; if (co >= 7) { co -= 7; ro += 1; }
            }
        }
        auto ldx = [&](int c) {
#pragma unroll
            for (int i = 0; i < 4; ++i)
                glds16(xsrc + i * (8 * 896) + c * 128 + (((lane & 7) ^ ((4 * i + (lane >> 4)) & 7)) * 16), lds0 + L_XS + w * 20480 + (c % 5) * 4096 + i * 1024);
        };
        ldx(0); ldx(1); ldx(2); ldx(3); ldx(4);
        u32x4 xt[8];
#pragma unroll
        for (int i = 0; i < 8; ++i) {
            const char* g = xsrc + (i & 3) * (8 * 896) + (5 + (i >> 2)) * 128 + (((lane & 7) ^ ((4 * (i & 3) + (lane >> 4)) & 7)) * 16);
            asm volatile("global_load_dwordx4 %0, %1, off" : "=v"(xt[i]) : "v"(g) : "memory");
        }
        BAR();
        const LAS char* xr = (const LAS char*)lds + L_XS + w * 20480 + r * 128;
        const int xk = (r >> 1) & 7;
        const int trow = 8 * h + ((lane & 15) >> 2), tcol = ((lane >> 4) & 1) * 32 + (lane & 3) * 8;
        const LAS char* wr = (const LAS char*)lds + L_WP + trow * 112 + tcol;
        f32x16 acc0 = f32x16{}, acc1 = f32x16{};
        bf16x8 xf[28], wf[8];
        WAITV(28); asm volatile("" : "+v"(genv)); arrive(cnt + C_W, lane); waitc(cnt + C_W, 4u);
        tag = __builtin_amdgcn_readfirstlane(genv) + 1u;
        STAMP(0, 1);
#pragma unroll
        for (int kk = 0; kk < 4; ++kk) { wf[2 * kk] = trfrag(wr + kk * 16 * 112, 112); wf[2 * kk + 1] = trfrag(wr + kk * 16 * 112 + 56, 112); }
        auto gemm = [&](auto isv_c) __attribute__((always_inline)) {
        constexpr bool ISV = decltype(isv_c)::value;
#pragma unroll
        for (int c = 0; c < 7; ++c) {
            if (c == 0) WAITV(24); else if (c == 1) WAITV(20); else if (c == 2) WAITV(16); else if (c == 3) WAITV(12); else if (c == 4) WAITV(8);
            else if (c == 5) {
                asm volatile("s_waitcnt vmcnt(0)" : "+v"(xt[0]), "+v"(xt[1]), "+v"(xt[2]), "+v"(xt[3]), "+v"(xt[4]), "+v"(xt[5]), "+v"(xt[6]), "+v"(xt[7]) :: "memory");
#pragma unroll
                for (int i = 0; i < 8; ++i) *(LAS u32x4*)(lds + L_XS + w * 20480 + i * 1024 + lane * 16) = xt[i];
                WAITL();
            }
#pragma unroll
            for (int kk = 0; kk < 4; ++kk) xf[4 * c + kk] = *(const LAS bf16x8*)(xr + (c % 5) * 4096 + (((2 * kk + h) ^ xk) * 16));
#define WNEXT if (c < 6) { wf[2 * kk] = trfrag(wr + ((c + 1) * 64 + kk * 16) * 112, 112); wf[2 * kk + 1] = trfrag(wr + ((c + 1) * 64 + kk * 16) * 112 + 56, 112); } SB();
            SB();
            if (ISV) {
#pragma unroll
                for (int kk = 0; kk < 4; ++kk) { acc0 = MFMA32(xf[4 * c + kk], wf[2 * kk], acc0); acc1 = MFMA32(xf[4 * c + kk], wf[2 * kk + 1], acc1); SB(); WNEXT }
            } else {
#pragma unroll
                for (int kk = 0; kk < 4; ++kk) { acc0 = MFMA32(wf[2 * kk], xf[4 * c + kk], acc0); acc1 = MFMA32(wf[2 * kk + 1], xf[4 * c + kk], acc1); SB(); WNEXT }
            }
        }
        };
        if (isv) gemm(BoolC<true>{}); else gemm(BoolC<false>{});
        STAMP(0, 2);
        {
            const int h0 = 2 * xcd, dst = WS_KV + (((h0 * 64 + ti * 4 + w) * 4 + (isv ? 2 : 0)) * 64 + lane) * 16;
#pragma unroll
            for (int j = 0; j < 2; ++j) {
                const f32x16 a = j ? acc1 : acc0;
                u32x4 f0, f1;
#pragma unroll
                for (int e = 0; e < 4; ++e) { f0[e] = cvtpk(a[2 * e], a[2 * e + 1]); f1[e] = cvtpk(a[8 + 2 * e], a[9 + 2 * e]); }
                if (isv) { if (r >= 28) { const unsigned v = r == 28 ? 0x3f803f80u : 0u; f0 = u32x4{v, v, v, v}; f1 = f0; } }
                else if (h) { f1[2] = 0x3f803f80u; f1[3] = 0x00003f80u; }
                st128(rs, dst + j * 262144, f0); st128(rs, dst + j * 262144 + 1024, f1);
            }
        }
        {
            const char* qs = (const char*)wqkv + ((56 * head) & ~15) + (size_t)(w * 112 + (lane >> 2)) * 2688 + (lane & 3) * 16;
#pragma unroll
            for (int i = 0; i < 7; ++i) glds16(qs + i * (16 * 2688), lds0 + L_XS + w * 20480 + 8192 + i * 1024);
        }
        WAITV(7);
        if (arrive_rtn(cnt + C_KV) == 3u && lane == 0) st32(rs, F_KV + (xcd * 32 + (bid >> 3)) * 128, tag);
        STAMP(0, 3);
        WAITV(0);
        arrive(cnt + C_WQ, lane); waitc(cnt + C_WQ, 4u);
        STAMP(0, 4);
        {
            const LAS char* qr = (const LAS char*)lds + L_XS + 8192 + trow * 64 + tcol + ((56 * head) & 15);
            f32x16 qa = f32x16{}, qb = f32x16{};
#pragma unroll
            for (int g = 0; g < 4; ++g) {
                bf16x8 wf[7];
#pragma unroll
                for (int kk = 0; kk < 7; ++kk) {
                    wf[kk] = trfrag(qr + g * 20480 + kk * 1024, 64);
                    const int ks = 7 * g + kk;
                    if (ks >= 16) xf[ks] = *(const LAS bf16x8*)(xr + ((ks >> 2) % 5) * 4096 + (((2 * (ks & 3) + h) ^ xk) * 16));
                }
                SB();
#pragma unroll
                for (int kk = 0; kk < 7; ++kk) { if (kk & 1) qb = MFMA32(wf[kk], xf[7 * g + kk], qb); else qa = MFMA32(wf[kk], xf[7 * g + kk], qa); }
            }
            qa = (qa + qb) * QSCALE;
            u32x4 f0, f1;
#pragma unroll
            for (int e = 0; e < 4; ++e) { f0[e] = cvtpk(qa[2 * e], qa[2 * e + 1]); f1[e] = cvtpk(qa[8 + 2 * e], qa[9 + 2 * e]); }
            if (h) { f1[2] = 0u; f1[3] = 0u; }
            q0 = BC8(f0); q1 = BC8(f1);
        }
        arrive(cnt + C_Q, lane);
        STAMP(0, 5);
        if (w == 0) { poll32(rs, F_KV + (xcd * 32 + (lane & 31)) * 128, tag); arrive(cnt + C_PK, lane); }
        else waitc(cnt + C_PK, 1u);
        waitc(cnt + C_Q, 4u);
        STAMP(0, 6);
#pragma unroll
        for (int i = 0; i < NT0; ++i) glds16c(kvsrc + (i * 4 + w) * 1024, lds0 + (i * 4 + w) * 1024);
        WAITV(12); arrive(cnt + C_T0, lane); waitc(cnt + C_T0, 4u);
        STAMP(0, 7); CSTAMP0;
        k0 = kv[0]; k1 = kv[64]; v1 = k0;
        sa = MFMA32(k0, q0, f32x16{}); sb = MFMA32(k1, q1, sa);
        q1 = set_ref(q1, xhalf_max(max16(sb)), h);
        waitc(cnt + C_B3, 4u);
        qs_[0] = q0; qs_[64] = q1;
        arrive(cnt + C_QS, lane);
        sa = MFMA32(k1, q1, sa);
        k0 = kv[256]; k1 = kv[256 + 64];
    } else {
        __builtin_amdgcn_s_setprio(2);
        const int b = bid, t = tid - 256;
        u32x4 wfrag;
        LAS cf* fb = (LAS cf*)(lds + L_F);
        LAS cf* f = fb + w * 7 * RSTR;
        const int l2 = lane < 56 ? lane : 55;
        const int n2 = 2 * t, pos = (n2 >> 6) * RSTR + (n2 & 63);
        const float rr = 0.70710678118654752f;
        const int s2 = l2 >> 3, r0 = l2 & 7, m1 = l2 / 7, s3 = l2 - 7 * m1;
        const int j = 8 * w + (lane >> 3), j0 = (lane >> 3) & 3, d0 = 2 * w + (lane >> 5);
        cf a[8], t256[8], t32[8];
        {
            unsigned xv[8], wv[8];
            const int tc = t < 224 ? t : 223;
#pragma unroll
            for (int i = 0; i < 8; ++i) xv[i] = *(const unsigned*)(bx + (size_t)(256 * i + b) * 448 + 2 * tc);
            const int item = bid * 112 + (t < 112 ? t : 111), l = item & 63, fr = item >> 6, hd = fr & 15, cb = fr >> 4;
            const int g = l >> 4, dd = 16 * (g >> 1) + 4 * (g & 1);
            const unsigned short* src = wo + (size_t)(hd * 28) * 448 + cb * 16 + (l & 15);
            if (w < 2) {
#pragma unroll
                for (int e = 0; e < 8; ++e) { const int d = dd + 8 * (e >> 2) + (e & 3); wv[e] = src[(d < 28 ? d : 0) * 448]; if (d >= 28) wv[e] = 0u; }
            }
            BAR();
#pragma unroll
            for (int i = 1; i < 8; ++i) { if (i < 7) t448[i] = tw(lane * i, 1.f / 448); t64[i] = tw(r0 * i, 1.f / 64); t256[i] = tw(j * i, 1.f / 256); t32[i] = tw(j0 * i, 1.f / 32); }
            tb1 = tw(b, 1.f / 2048); tb2 = tw(2 * b, 1.f / 2048); tb3 = tw(3 * b, 1.f / 2048); tb4 = tw(b, 1.f / 512);
#pragma unroll
            for (int i = 1; i < 8; ++i) { if (i < 7) asm volatile("" : "+v"(t448[i])); asm volatile("" : "+v"(t64[i]), "+v"(t256[i]), "+v"(t32[i])); }
            asm volatile("" : "+v"(tb1), "+v"(tb2), "+v"(tb3), "+v"(tb4));
            if (t < 224) {
                cf v[8];
#pragma unroll
                for (int i = 0; i < 8; ++i) v[i] = cf{bflo(xv[i]), bfhi(xv[i])};
                cf p0 = v[0] + v[4], m0 = v[0] - v[4], p1 = v[1] + v[5], n1 = v[1] - v[5];
                cf p2 = v[2] + v[6], m2 = v[2] - v[6], p3 = v[3] + v[7], m3 = v[3] - v[7];
                cf e0 = p0 + p2, e1 = p1 + p3;
                cf z0 = e0 + e1, z4 = e0 - e1;
                cf z2r = p0 - p2, z2i = p3 - p1;
                cf qa = (n1 - m3) * rr, qb = (n1 + m3) * rr;
                cf z1r = m0 + qa, z1i = -m2 - qb, z3r = m0 - qa, z3i = m2 - qb;
                const cf w1 = tb1, w2 = tb2, w3 = tb3;
                cf a0, a1;
                *(LAS f32x4*)(fb + pos) = f32x4{z0.x, z4.x, z0.y, z4.y};
                a0 = cmul<-1>(cf{z1r.x, z1i.x}, w1); a1 = cmul<-1>(cf{z1r.y, z1i.y}, w1);
                *(LAS f32x4*)(fb + 7 * RSTR + pos) = f32x4{a0.x, a0.y, a1.x, a1.y};
                a0 = cmul<-1>(cf{z2r.x, z2i.x}, w2); a1 = cmul<-1>(cf{z2r.y, z2i.y}, w2);
                *(LAS f32x4*)(fb + 14 * RSTR + pos) = f32x4{a0.x, a0.y, a1.x, a1.y};
                a0 = cmul<-1>(cf{z3r.x, z3i.x}, w3); a1 = cmul<-1>(cf{z3r.y, z3i.y}, w3);
                *(LAS f32x4*)(fb + 21 * RSTR + pos) = f32x4{a0.x, a0.y, a1.x, a1.y};
            }
            arrive(cnt + C_B0, lane);
            WAITV(0); asm volatile("" : "+v"(genv));
            wfrag = u32x4{wv[0] | (wv[1] << 16), wv[2] | (wv[3] << 16), wv[4] | (wv[5] << 16), wv[6] | (wv[7] << 16)};
            waitc(cnt + C_B0, 4u);
        }
        STAMP(0, 1);
#pragma unroll
        for (int p = 0; p < 7; ++p) a[p] = f[p * RSTR + lane];
        dft7<-1>(a);
#pragma unroll
        for (int s = 1; s < 7; ++s) a[s] = cmul<-1>(a[s], t448[s]);
#pragma unroll
        for (int s = 0; s < 7; ++s) f[s * RSTR + lane] = a[s];
        WAITL();
#pragma unroll
        for (int i = 0; i < 8; ++i) a[i] = f[s2 * RSTR + 8 * i + r0];
        dft8<-1>(a);
#pragma unroll
        for (int i = 1; i < 8; ++i) a[i] = cmul<-1>(a[i], t64[i]);
        if (lane < 56) {
#pragma unroll
            for (int i = 0; i < 8; ++i) f[s2 * RSTR + 8 * i + r0] = a[i];
        }
        WAITL();
#pragma unroll
        for (int i = 0; i < 8; ++i) a[i] = f[s3 * RSTR + 8 * m1 + i];
        dft8<-1>(a);
        {
            LAS cf* y = (LAS cf*)(lds + L_YA);
            if (w != 0) {
                if (lane < 56) {
#pragma unroll
                    for (int i = 0; i < 4; ++i) y[w * 225 + lane + 56 * i] = a[i];
#pragma unroll
                    for (int i = 4; i < 8; ++i) y[(8 - w) * 225 + 448 - lane - 56 * i] = cconj(a[i]);
                    if (lane == 0) { y[w * 225 + 224] = a[4]; y[(8 - w) * 225] = cconj(a[0]); }
                }
            } else {
                WAITL();
                if (lane < 56) {
#pragma unroll
                    for (int i = 0; i < 8; ++i) f[lane + 56 * i] = a[i];
                }
                if (lane == 0) f[448] = a[0];
                WAITL();
                const cf w4 = cf{tb4.y, -tb4.x} * 0.5f;
#pragma unroll
                for (int i = 0; i < 4; ++i) {
                    const int k = lane + 64 * i;
                    if (k <= 224) {
                        const cf u = f[k], v = cconj(f[448 - k]);
                        y[k] = (u + v) * 0.5f;
                        y[4 * 225 + k] = cmul<-1>(u - v, w4);
                    }
                }
            }
            arrive(cnt + C_B1, lane); waitc(cnt + C_B1, 4u);
#pragma unroll
            for (int i = 0; i < 4; ++i) { const int e = t + 256 * i; if (e < 900) st128(rs, WS_Y1 + b * (NQ * 8) + e * 16, ((LAS u32x4*)y)[e]); }
        }
        tag = __builtin_amdgcn_readfirstlane(genv) + 1u;
        STAMP(0, 2);
        publish(rs, cnt + C_B3, F_Y1 + bid * 4, tag, lane, 8);
        STAMP(0, 3);
        {
            const int grp = (bid & 7) * 29 + (bid >> 3);
            const bool act = (bid >> 3) < 29 && grp < 225;
            const int col = lane & 7, q = (act ? grp : 0) * 8 + col, c = q / 225, k2 = q - c * 225, cs = c > 4 ? c - 8 : c;
            if (t < 112) st128(rs, WS_WO + (bid * 112 + t) * 16, wfrag);
            {
                const int dst = WS_OF + ((((ti * 4 + w) * 16 + head) * 2) * 64 + lane) * 16;
                st128(rs, dst, u32x4{}); st128(rs, dst + 1024, u32x4{});
            }
            unsigned fr_[8], fi_[8];
#pragma unroll
            for (int i = 0; i < 8; ++i) {
                const int d = d0 + 8 * (j0 + 4 * (i >> 2)) + 64 * (i & 3), k1 = (cs + 8 * d) & 2047;
                fr_[i] = fwr[k1 * 225 + k2]; fi_[i] = fwi[k1 * 225 + k2];
            }
            waitc(cnt + C_PK, 1u); waitc(cnt + C_Q, 4u);
            asm volatile("s_waitcnt vmcnt(0)" : "+v"(fr_[0]), "+v"(fr_[1]), "+v"(fr_[2]), "+v"(fr_[3]), "+v"(fr_[4]), "+v"(fr_[5]), "+v"(fr_[6]), "+v"(fr_[7]),
                         "+v"(fi_[0]), "+v"(fi_[1]), "+v"(fi_[2]), "+v"(fi_[3]), "+v"(fi_[4]), "+v"(fi_[5]), "+v"(fi_[6]), "+v"(fi_[7]) :: "memory");
            if (w == 0) {
                poll128(rs, F_Y1 + xcd * FREP + lane * 16, tag);
                arrive(cnt + C_B2, lane);
            } else waitc(cnt + C_B2, 1u);
            STAMP(0, 4);
            LAS cf* mb = fb;
#pragma unroll
            for (int i = 0; i < 8; ++i) a[i] = ld64(rs, WS_Y1 + ((32 * i + j) * NQ + q) * 8);
            asm volatile("" : "+v"(a[0]), "+v"(a[1]), "+v"(a[2]), "+v"(a[3]), "+v"(a[4]), "+v"(a[5]), "+v"(a[6]), "+v"(a[7]));
#pragma unroll 4
            for (int i = NT0; i < 32; ++i) glds16c(kvsrc + (i * 4 + w) * 1024, lds0 + (i * 4 + w) * 1024);
            if (act) {
                dft8<-1>(a);
                STAMP(0, 5);
#pragma unroll
                for (int i = 1; i < 8; ++i) a[i] = cmul<-1>(a[i], t256[i]);
#pragma unroll
                for (int i = 0; i < 8; ++i) mb[(i * 32 + j) * 8 + col] = a[i];
                arrive(cnt + C_B4, lane); waitc(cnt + C_B4, 4u);
#pragma unroll
                for (int i = 0; i < 8; ++i) a[i] = mb[(d0 * 32 + 4 * i + j0) * 8 + col];
                dft8<-1>(a);
#pragma unroll
                for (int i = 1; i < 8; ++i) a[i] = cmul<-1>(a[i], t32[i]);
#pragma unroll
                for (int i = 0; i < 8; ++i) mb[(d0 * 32 + 4 * i + j0) * 8 + col] = a[i];
                WAITL();
#pragma unroll
                for (int i = 0; i < 8; ++i) a[i] = mb[(d0 * 32 + 4 * (j0 + 4 * (i >> 2)) + (i & 3)) * 8 + col];
                dft4<-1>(a[0], a[1], a[2], a[3]); dft4<-1>(a[4], a[5], a[6], a[7]);
#pragma unroll
                for (int i = 0; i < 8; ++i) a[i] = cmul<1>(a[i], cf{bflo(fi_[i]), bflo(fr_[i])});
                dft4<1>(a[0], a[1], a[2], a[3]); dft4<1>(a[4], a[5], a[6], a[7]);
#pragma unroll
                for (int i = 0; i < 8; ++i) mb[(d0 * 32 + 4 * (j0 + 4 * (i >> 2)) + (i & 3)) * 8 + col] = a[i];
                WAITL();
#pragma unroll
                for (int i = 0; i < 8; ++i) a[i] = mb[(d0 * 32 + 4 * i + j0) * 8 + col];
#pragma unroll
                for (int i = 1; i < 8; ++i) a[i] = cmul<1>(a[i], t32[i]);
                dft8<1>(a);
#pragma unroll
                for (int i = 0; i < 8; ++i) mb[(d0 * 32 + 4 * i + j0) * 8 + col] = a[i];
                WAITV(0);
                arrive(cnt + C_B5, lane); waitc(cnt + C_B5, 4u);
#pragma unroll
                for (int i = 0; i < 8; ++i) a[i] = mb[(i * 32 + j) * 8 + col];
#pragma unroll
                for (int i = 1; i < 8; ++i) a[i] = cmul<1>(a[i], t256[i]);
                dft8<1>(a);
#pragma unroll
                for (int i = 0; i < 8; ++i) st64(rs, WS_Y2 + ((32 * i + j) * NQ + q) * 8, a[i]);
            }
        }
        STAMP(0, 6);
        if (!((bid >> 3) < 29 && (bid & 7) * 29 + (bid >> 3) < 225)) { WAITV(0); arrive(cnt + C_B5, lane); waitc(cnt + C_B5, 4u); }
        waitc(cnt + C_QS, 4u); waitc(cnt + C_T1, 4u);
        __builtin_amdgcn_s_setprio(0);
        q0 = qs_[0]; q1 = qs_[64];
        k0 = kv[7 * 512]; k1 = kv[7 * 512 + 64]; v1 = k0;
        sa = MFMA32(k0, q0, f32x16{}); sa = MFMA32(k1, q1, sa);
        k0 = kv[7 * 512 + 256]; k1 = kv[7 * 512 + 256 + 64];
        publish(rs, cnt + C_R0, F_Y2 + bid * 4, tag, lane, 8);
        arrive(cnt + C_M, lane);
        STAMP(0, 7); CSTAMP0;
    }
    {
        int p = sp ? 7 : 0;
#pragma unroll 1
        for (int m = sp ? 0 : -6; m < 13; ++m) {
            if (m >= 0) {
                WAITV(2);
                asm volatile("s_mov_b64 exec, 1\n\tds_add_u32 %0, %1 offset:4\n\ts_mov_b64 exec, -1" :: "v"(cnt + C_M + m), "v"(1u) : "memory");
                waitc(cnt + C_M + m, 8u);
                if (m < 8) {
                    glds16c(kvsrc + ((16 + 2 * m) * 8 + wid) * 1024, lds0 + (2 * m * 8 + wid) * 1024);
                    glds16c(kvsrc + ((17 + 2 * m) * 8 + wid) * 1024, lds0 + ((2 * m + 1) * 8 + wid) * 1024);
                } else WAITV(0);
            } else if (m == -5) { WAITV(0); arrive(cnt + C_T1, lane); waitc(cnt + C_T1, 4u); }
            else if (m == -4) arrive(cnt + C_M, lane);
            const int pn = p + (m < 0 ? 1 : 2);
            const LAS bf16x8* pv = kv + (p & 15) * 512;
            const LAS bf16x8* pk = kv + (pn & 15) * 512;
            STEP(sa, sb, pv, pk) STEP(sb, sa, pv + 256, pk + 256)
            p = pn;
        }
        p1[2] = cvtpk(e0, e1); p1[3] = cvtpk(e2, e3);
        o = MFMA32(v1, BC8(p1), o);
    }
    STAMP(0, 8); CSTAMP1;
    LAS f32x4* ox = (LAS f32x4*)(lds + L_F) + w * 256 + lane;
    if (wid < 4) {
        waitc(cnt + C_OX, 4u);
#pragma unroll
        for (int e = 0; e < 4; ++e) { const f32x4 v = ox[e * 64]; o[4 * e] += v[0]; o[4 * e + 1] += v[1]; o[4 * e + 2] += v[2]; o[4 * e + 3] += v[3]; }
        arrive(cnt + C_OY, lane);
        float l = __shfl(o[12], r + 32);
        float chk = 0.f;
#pragma unroll
        for (int e = 0; e < 16; ++e) chk += o[e] * 0.f;
        if (__any(!(l > 0.25f && l < 1.0e18f && chk == 0.f))) {
            const int g = WS_KV + head * 262144 + lane * 16;
            q1 = set_ref(q1, 0.f, h);
            float m = -INFINITY;
#pragma unroll 1
            for (int t = 0; t < 64; ++t) {
                f32x16 s = MFMA32(BC8(ld128(rs, g + t * 4096)), q0, f32x16{});
                s = MFMA32(BC8(ld128(rs, g + t * 4096 + 1024)), q1, s);
                m = __builtin_fmaxf(m, max16(s));
            }
            q1 = set_ref(q1, xhalf_max(m), h);
            o = f32x16{};
#pragma unroll 1
            for (int t = 0; t < 64; ++t) {
                f32x16 s = MFMA32(BC8(ld128(rs, g + t * 4096)), q0, f32x16{});
                s = MFMA32(BC8(ld128(rs, g + t * 4096 + 1024)), q1, s);
#pragma unroll
                for (int e = 0; e < 4; ++e) { p0[e] = cvtpk(EX(s[2 * e]), EX(s[2 * e + 1])); p1[e] = cvtpk(EX(s[8 + 2 * e]), EX(s[9 + 2 * e])); }
                o = MFMA32(BC8(ld128(rs, g + t * 4096 + 2048)), BC8(p0), o);
                o = MFMA32(BC8(ld128(rs, g + t * 4096 + 3072)), BC8(p1), o);
            }
            l = __shfl(o[12], r + 32);
        }
        const float inv = 1.0f / l;
#pragma unroll
        for (int e = 0; e < 4; ++e) { p0[e] = cvtpk(o[2 * e] * inv, o[2 * e + 1] * inv); p1[e] = cvtpk(o[8 + 2 * e] * inv, o[9 + 2 * e] * inv); }
        {
            const int dst = WS_OF + ((((ti * 4 + w) * 16 + head) * 2) * 64 + lane) * 16;
            st128(rs, dst, p0); st128(rs, dst + 1024, p1);
        }
        publish(rs, cnt + C_O, F_O + (ti * 16 + head) * 128, tag, lane, 1);
        STAMP(0, 9);
        waitc(cnt + C_PY, 1u);
        bf16x8 wa[16], wb[16];
        const int cb0 = cq * 7 + w, cb1 = cq * 7 + (w < 3 ? w + 4 : 6);
#pragma unroll
        for (int i = 0; i < 16; ++i) { wa[i] = BC8(ld128(rs, WS_WO + ((cb0 * 16 + i) * 64 + lane) * 16)); wb[i] = BC8(ld128(rs, WS_WO + ((cb1 * 16 + i) * 64 + lane) * 16)); }
        if (w == 0) { poll32(rs, F_O + ((otile >> 2) * 16 + (lane & 15)) * 128, tag); arrive(cnt + C_PO, lane); }
        else waitc(cnt + C_PO, 1u);
        STAMP(0, 10);
        u32x4 ov[8];
#pragma unroll
        for (int i = 0; i < 8; ++i) ov[i] = ld128(rs, WS_OF + (otile * 2048 + (i * 4 + w) * 64 + lane) * 16);
        waitc(cnt + C_O, 4u);
#pragma unroll
        for (int i = 0; i < 8; ++i) ((LAS u32x4*)lds)[(i * 4 + w) * 64 + lane] = ov[i];
        arrive(cnt + C_OS, lane); waitc(cnt + C_OS, 4u);
        STAMP(0, 11);
        {
            const int g = lane >> 4;
            const LAS bf16x8* op = (const LAS bf16x8*)lds + (g >> 1) * 64 + (g & 1) * 32 + (lane & 15);
            f32x4 c0 = f32x4{}, c1 = f32x4{}, c2 = f32x4{}, c3 = f32x4{};
#pragma unroll
            for (int i = 0; i < 16; ++i) {
                const bf16x8 a0 = op[i * 128], a1 = op[i * 128 + 16];
                c0 = MFMA16(wa[i], a0, c0); c1 = MFMA16(wa[i], a1, c1); c2 = MFMA16(wb[i], a0, c2); c3 = MFMA16(wb[i], a1, c3);
            }
            unsigned short* dst = out + (size_t)(otile * 32 + (lane & 15)) * 896 + g * 4;
            *(u32x2*)(dst + cb0 * 16) = u32x2{cvtpk(c0[0], c0[1]), cvtpk(c0[2], c0[3])};
            *(u32x2*)(dst + cb0 * 16 + 16 * 896) = u32x2{cvtpk(c1[0], c1[1]), cvtpk(c1[2], c1[3])};
            if (w < 3) {
                *(u32x2*)(dst + cb1 * 16) = u32x2{cvtpk(c2[0], c2[1]), cvtpk(c2[2], c2[3])};
                *(u32x2*)(dst + cb1 * 16 + 16 * 896) = u32x2{cvtpk(c3[0], c3[1]), cvtpk(c3[2], c3[3])};
            }
        }
        STAMP(0, 12); STAMP_FLUSH(0);
    } else {
#pragma unroll
        for (int e = 0; e < 4; ++e) ox[e * 64] = f32x4{o[4 * e], o[4 * e + 1], o[4 * e + 2], o[4 * e + 3]};
        arrive(cnt + C_OX, lane);
        const int b = bid, t = tid - 256;
        LAS cf* fb = (LAS cf*)(lds + L_F);
        LAS cf* f = fb + w * 7 * RSTR;
        const int l2 = lane < 56 ? lane : 55;
        const int n2 = 2 * t, pos = (n2 >> 6) * RSTR + (n2 & 63);
        const float rr = 0.70710678118654752f;
        const int s2 = l2 >> 3, r0 = l2 & 7, m1 = l2 / 7, s3 = l2 - 7 * m1;
        cf a[8];
        if (w == 0) {
            poll128(rs, F_Y2 + xcd * FREP + lane * 16, tag);
            arrive(cnt + C_PY, lane);
            if (bid == 0 && lane == 0) st32(rs, F_GEN, tag);
        } else waitc(cnt + C_PY, 1u);
        {
            const int y = WS_Y2 + b * (NQ * 8);
            cf z[8];
            const int ya = y + (w * 225) * 8, yb = y + ((w ? 8 - w : 0) * 225) * 8;
#pragma unroll
            for (int i = 0; i < 8; ++i) a[i] = ld64(rs, i < 4 ? ya + (l2 + 56 * i) * 8 : yb + (448 - l2 - 56 * i) * 8);
            if (w) { z[0] = ld64(rs, yb); z[4] = ld64(rs, ya + 224 * 8); }
            else {
#pragma unroll
                for (int i = 0; i < 8; ++i) z[i] = ld64(rs, y + (900 + (i < 4 ? l2 + 56 * i : 448 - l2 - 56 * i)) * 8);
            }
            waitc(cnt + C_OY, 4u);
            STAMP(0, 9);
#pragma unroll
            for (int i = 4; i < 8; ++i) a[i] = cconj(a[i]);
            if (w) { if (lane == 0) { a[0] = (a[0] + cconj(z[0])) * 0.5f; a[4] = (a[4] + z[4]) * 0.5f; } }
            else {
                const cf w4 = tb4;
#pragma unroll
                for (int i = 0; i < 8; ++i) {
                    cf z4 = cmul<1>(z[i], w4);
                    if (i >= 4) z4 = cconj(z4);
                    if ((i & 3) == 0 && lane == 0) { a[i].y = 0.f; z4.y = 0.f; }
                    a[i] = addrot<1>(a[i], z4);
                }
            }
        }
        dft8<1>(a);
        if (lane < 56) {
#pragma unroll
            for (int i = 0; i < 8; ++i) f[s3 * RSTR + 8 * m1 + i] = a[i];
        }
        WAITL();
#pragma unroll
        for (int i = 0; i < 8; ++i) a[i] = f[s2 * RSTR + 8 * i + r0];
#pragma unroll
        for (int i = 1; i < 8; ++i) a[i] = cmul<1>(a[i], t64[i]);
        dft8<1>(a);
        if (lane < 56) {
#pragma unroll
            for (int i = 0; i < 8; ++i) f[s2 * RSTR + 8 * i + r0] = a[i];
        }
        WAITL();
#pragma unroll
        for (int s = 0; s < 7; ++s) a[s] = f[s * RSTR + lane];
#pragma unroll
        for (int s = 1; s < 7; ++s) a[s] = cmul<1>(a[s], t448[s]);
        dft7<1>(a);
#pragma unroll
        for (int p = 0; p < 7; ++p) f[p * RSTR + lane] = a[p];
        arrive(cnt + C_B6, lane); waitc(cnt + C_B6, 4u);
        if (t < 224) {
            const float sc = 1.0f / (2048.0f * 448.0f), sc2 = 2.f * sc;
            const f32x4 u0 = *(LAS f32x4*)(fb + pos), u1 = *(LAS f32x4*)(fb + 7 * RSTR + pos), u2 = *(LAS f32x4*)(fb + 14 * RSTR + pos), u3 = *(LAS f32x4*)(fb + 21 * RSTR + pos);
            const cf w1 = tb1 * sc2, w2 = tb2 * sc2, w3 = tb3 * sc2;
            const cf z0 = cf{u0[0], u0[2]} * sc, z4 = cf{u0[1], u0[3]} * sc;
            cf a0 = cmul<1>(cf{u1[0], u1[1]}, w1), a1 = cmul<1>(cf{u1[2], u1[3]}, w1);
            const cf z1r = cf{a0.x, a1.x}, z1i = cf{a0.y, a1.y};
            a0 = cmul<1>(cf{u2[0], u2[1]}, w2); a1 = cmul<1>(cf{u2[2], u2[3]}, w2);
            const cf z2r = cf{a0.x, a1.x}, z2i = cf{a0.y, a1.y};
            a0 = cmul<1>(cf{u3[0], u3[1]}, w3); a1 = cmul<1>(cf{u3[2], u3[3]}, w3);
            const cf z3r = cf{a0.x, a1.x}, z3i = cf{a0.y, a1.y};
            const cf e0 = z0 + z4, e1 = z0 - z4;
            const cf p0 = e0 + z2r, p2 = e0 - z2r, p1 = e1 - z2i, p3 = e1 + z2i;
            const cf m0 = z1r + z3r, m2 = z3i - z1i;
            const cf qa = (z1r - z3r) * rr, qb = (z1i + z3i) * rr;
            const cf mm1 = qa - qb, m3 = -qa - qb;
            const cf yv[8] = {p0 + m0, p1 + mm1, p2 + m2, p3 + m3, p0 - m0, p1 - mm1, p2 - m2, p3 - m3};
#pragma unroll
            for (int i = 0; i < 8; ++i) *(unsigned*)(out + (size_t)(256 * i + b) * 896 + 448 + n2) = cvtpk(yv[i].x, yv[i].y);
        }
        STAMP(0, 10); STAMP_FLUSH(0);
    }
}

extern "C" void kernel_launch(void* const* d_in, const int* in_sizes, int n_in, void* d_out, int out_size, void* d_ws,
                              size_t ws_size, hipStream_t stream) {
    (void)hipFuncSetAttribute((const void*)fused_kernel, hipFuncAttributeMaxDynamicSharedMemorySize, L_END);
    fused_kernel<<<256, 512, L_END, stream>>>((const unsigned short*)d_in[0], (const unsigned short*)d_in[1], (const unsigned short*)d_in[2],
                                              (const unsigned short*)d_in[3], (const unsigned short*)d_in[4], (const unsigned short*)d_in[5],
                                              (unsigned short*)d_out, (char*)d_ws);
}
```

```cpp
#include <hip/hip_runtime.h>
#include <hip/hip_bf16.h>
#include <stdint.h>

typedef __attribute__((ext_vector_type(8))) short bf16x8;
typedef __attribute__((ext_vector_type(4))) short s16x4;
typedef __attribute__((ext_vector_type(16))) float f32x16;
typedef __attribute__((ext_vector_type(4))) float f32x4;
typedef __attribute__((ext_vector_type(4))) unsigned u32x4;
typedef __attribute__((ext_vector_type(2))) unsigned u32x2;
typedef __attribute__((ext_vector_type(2))) float cf;
#define LAS __attribute__((address_space(3)))
#define NOPK __attribute__((target("no-packed-fp32-ops")))
#define DEVI __device__ __forceinline__
#define MFMA32(a, b, c) __builtin_amdgcn_mfma_f32_32x32x16_bf16(a, b, c, 0, 0, 0)
#define MFMA16(a, b, c) __builtin_amdgcn_mfma_f32_16x16x32_bf16(a, b, c, 0, 0, 0)

constexpr int NQ = 1800;
constexpr int F_GEN = 0, F_KV = 256, F_O = F_KV + 256 * 128, F_Y1 = F_O + 256 * 128, FREP = 1024 + 256, F_Y2 = F_Y1 + 8 * FREP;
constexpr int WS_KV = 131072;
constexpr int WS_WO = WS_KV + 16 * 64 * 4 * 1024;
constexpr int WS_OF = WS_WO + 28 * 16 * 1024;
constexpr int WS_Y1 = WS_OF + 64 * 16 * 2 * 1024;
constexpr int WS_Y2 = WS_Y1;
constexpr int WS_END = WS_Y1 + 256 * NQ * 8;
constexpr float QSCALE = 0.18898223650461362f * 1.4426950408889634f;

typedef __attribute__((ext_vector_type(2))) __bf16 bf16x2_t;
DEVI unsigned cvtpk(float lo, float hi) { return __builtin_bit_cast(unsigned, __builtin_convertvector(cf{lo, hi}, bf16x2_t)); }
DEVI float bflo(unsigned u) { return __uint_as_float(u << 16); }
DEVI float bfhi(unsigned u) { return __uint_as_float(u & 0xffff0000u); }
DEVI void glds16(const void* g, unsigned lds_base) {
    unsigned sv;
    asm volatile("s_mov_b32 %0, m0\n\ts_mov_b32 m0, %2\n\ts_nop 0\n\tglobal_load_lds_dwordx4 %1, off\n\ts_mov_b32 m0, %0"
                 : "=&s"(sv) : "v"(g), "s"(__builtin_amdgcn_readfirstlane(lds_base)) : "memory");
}
#define WAITV(N) asm volatile("s_waitcnt vmcnt(" #N ")" ::: "memory")
#define WAITL() asm volatile("s_waitcnt lgkmcnt(0)" ::: "memory")
#define BAR() asm volatile("s_waitcnt lgkmcnt(0)\n\ts_barrier" ::: "memory")

#define STAMP(K, I)
#define STAMP_DECL
#define STAMP_FLUSH(K)
template <int DIR> DEVI cf cmul(cf a, cf w) {
    return DIR > 0 ? cf{__builtin_fmaf(a.x, w.y, -a.y * w.x), __builtin_fmaf(a.y, w.y, a.x * w.x)}
                   : cf{__builtin_fmaf(a.x, w.y, a.y * w.x), __builtin_fmaf(a.y, w.y, -a.x * w.x)};
}
template <int DIR> DEVI cf addrot(cf t, cf x) { return DIR > 0 ? cf{t.x - x.y, t.y + x.x} : cf{t.x + x.y, t.y - x.x}; }
DEVI cf cconj(cf a) { return cf{a.x, -a.y}; }
DEVI cf tw(int num, float inv_den) {
    const float x = (float)num * inv_den;
    return cf{__builtin_amdgcn_sinf(x), __builtin_amdgcn_cosf(x)};
}
template <int DIR> DEVI void dft4(cf& a0, cf& a1, cf& a2, cf& a3) {
    cf t0 = a0 + a2, t1 = a0 - a2, t2 = a1 + a3, t3 = a1 - a3;
    a0 = t0 + t2; a2 = t0 - t2; a1 = addrot<DIR>(t1, t3); a3 = addrot<-DIR>(t1, t3);
}
template <int DIR> DEVI void dft8(cf* a) {
    cf e0 = a[0], e1 = a[2], e2 = a[4], e3 = a[6], o0 = a[1], o1 = a[3], o2 = a[5], o3 = a[7];
    dft4<DIR>(e0, e1, e2, e3); dft4<DIR>(o0, o1, o2, o3);
    const float r = 0.70710678118654752f;
    o1 = addrot<DIR>(o1, o1); o3 = addrot<-DIR>(o3, o3);
    a[0] = e0 + o0; a[4] = e0 - o0; a[1] = e1 + o1 * r; a[5] = e1 - o1 * r;
    a[2] = addrot<DIR>(e2, o2); a[6] = addrot<-DIR>(e2, o2); a[3] = e3 - o3 * r; a[7] = e3 + o3 * r;
}
template <int DIR> DEVI void dft7(cf* a) {
    const float c1 = 0.62348980185873353f, c2 = -0.22252093395631440f, c3 = -0.90096886790241913f;
    const float s1 = 0.78183148246802981f, s2 = 0.97492791218182361f, s3 = 0.43388373911755812f;
    cf t1 = a[1] + a[6], t2 = a[2] + a[5], t3 = a[3] + a[4], u1 = a[1] - a[6], u2 = a[2] - a[5], u3 = a[3] - a[4];
    cf A1 = a[0] + t1 * c1 + t2 * c2 + t3 * c3, A2 = a[0] + t1 * c2 + t2 * c3 + t3 * c1, A3 = a[0] + t1 * c3 + t2 * c1 + t3 * c2;
    cf B1 = u1 * s1 + u2 * s2 + u3 * s3, B2 = u1 * s2 - u2 * s3 - u3 * s1, B3 = u1 * s3 - u2 * s1 + u3 * s2;
    a[0] = a[0] + t1 + t2 + t3;
    a[1] = addrot<DIR>(A1, B1); a[6] = addrot<-DIR>(A1, B1); a[2] = addrot<DIR>(A2, B2); a[5] = addrot<-DIR>(A2, B2);
    a[3] = addrot<DIR>(A3, B3); a[4] = addrot<-DIR>(A3, B3);
}
constexpr int RSTR = 72;
constexpr int RFB = 7 * RSTR * 8;

#define CSTAMP0
#define CSTAMP1
typedef __amdgpu_buffer_rsrc_t rsrc_t;
DEVI void st128(rsrc_t r, int off, u32x4 v) { __builtin_amdgcn_raw_buffer_store_b128(v, r, off, 0, 16); }
DEVI void st64(rsrc_t r, int off, cf v) { __builtin_amdgcn_raw_buffer_store_b64(__builtin_bit_cast(u32x2, v), r, off, 0, 16); }
DEVI void st32(rsrc_t r, int off, unsigned v) { __builtin_amdgcn_raw_buffer_store_b32(v, r, off, 0, 16); }
DEVI u32x4 ld128(rsrc_t r, int off) { return __builtin_amdgcn_raw_buffer_load_b128(r, off, 0, 16); }
DEVI cf ld64(rsrc_t r, int off) { return __builtin_bit_cast(cf, __builtin_amdgcn_raw_buffer_load_b64(r, off, 0, 16)); }
DEVI unsigned ld32(rsrc_t r, int off) { return __builtin_amdgcn_raw_buffer_load_b32(r, off, 0, 16); }
DEVI void glds16c(const void* g, unsigned lds_base) {
    unsigned sv;
    asm volatile("s_mov_b32 %0, m0\n\ts_mov_b32 m0, %2\n\ts_nop 0\n\tglobal_load_lds_dwordx4 %1, off sc1\n\ts_mov_b32 m0, %0"
                 : "=&s"(sv) : "v"(g), "s"(__builtin_amdgcn_readfirstlane(lds_base)) : "memory");
}
#define FENCE() asm volatile("" ::: "memory")
constexpr int SPIN_CAP = 1 << 18;
DEVI void arrive(LAS unsigned* c, int) {
    asm volatile("s_waitcnt lgkmcnt(0)\n\ts_mov_b64 exec, 1\n\tds_add_u32 %0, %1\n\ts_mov_b64 exec, -1" :: "v"(c), "v"(1u) : "memory");
}
DEVI unsigned arrive_rtn(LAS unsigned* c) {
    unsigned v, r;
    asm volatile("s_waitcnt lgkmcnt(0)\n\ts_mov_b64 exec, 1\n\tds_add_rtn_u32 %0, %2, %3\n\ts_waitcnt lgkmcnt(0)\n\tv_readfirstlane_b32 %1, %0\n\ts_mov_b64 exec, -1"
                 : "=&v"(v), "=s"(r) : "v"(c), "v"(1u) : "memory");
    return r;
}
DEVI void waitc(LAS unsigned* c, unsigned n) {
    unsigned v, sv, it;
    asm volatile("s_mov_b32 %2, 0x200000\n"
                 "1:\n\tds_read_b32 %0, %3\n\ts_waitcnt lgkmcnt(0)\n\tv_readfirstlane_b32 %1, %0\n\ts_cmp_ge_u32 %1, %4\n\ts_cbranch_scc1 2f\n\t"
                 "s_sleep 3\n\ts_sub_u32 %2, %2, 1\n\ts_cbranch_scc0 1b\n2:"
                 : "=&v"(v), "=&s"(sv), "=&s"(it) : "v"(c), "s"(n) : "memory", "scc");
}
DEVI void publish(rsrc_t r, LAS unsigned* c, int flag_off, unsigned tag, int lane, int nrep) {
    WAITV(0);
    if (arrive_rtn(c) == 3u && lane < nrep) st32(r, flag_off + lane * FREP, tag);
}
DEVI void poll32(rsrc_t r, int off, unsigned tag) {
#pragma unroll 1
    for (int i = 0; i < SPIN_CAP; ++i) { if (__builtin_amdgcn_ballot_w64(ld32(r, off) != tag) == 0ull) break; __builtin_amdgcn_s_sleep(2); FENCE(); }
    FENCE();
}
DEVI void poll128(rsrc_t r, int off, unsigned tag) {
#pragma unroll 1
    for (int i = 0; i < SPIN_CAP; ++i) {
        const u32x4 v = ld128(r, off);
        if (__builtin_amdgcn_ballot_w64(!(v[0] == tag && v[1] == tag && v[2] == tag && v[3] == tag)) == 0ull) break;
        __builtin_amdgcn_s_sleep(10); FENCE();
    }
    FENCE();
}

DEVI float xhalf_max(float m) {
    auto rr = __builtin_amdgcn_permlane32_swap(__float_as_uint(m), __float_as_uint(m), false, false);
    return __builtin_fmaxf(__uint_as_float(rr[0]), __uint_as_float(rr[1]));
}
DEVI float hi_half(float m) {
    auto rr = __builtin_amdgcn_permlane32_swap(__float_as_uint(m), __float_as_uint(m), false, false);
    return __uint_as_float(rr[1]);
}
DEVI float max16(const f32x16& s) {
    float m = __builtin_fmaxf(s[0], s[1]);
#pragma unroll
    for (int i = 2; i < 16; ++i) m = __builtin_fmaxf(m, s[i]);
    return m;
}
DEVI bf16x8 set_ref(bf16x8 q1, float m, int h) {
    const float nm = -m;
    const unsigned b0 = __float_as_uint(nm) & 0xffff0000u; const float r1 = nm - __uint_as_float(b0);
    const unsigned b1 = __float_as_uint(r1) & 0xffff0000u; const float r2 = r1 - __uint_as_float(b1);
    const unsigned b2 = __float_as_uint(r2) & 0xffff0000u;
    u32x4 u = __builtin_bit_cast(u32x4, q1);
    if (h) { u[2] = (b0 >> 16) | b1; u[3] = b2 >> 16; }
    return __builtin_bit_cast(bf16x8, u);
}
DEVI bf16x8 trfrag(const LAS char* p, int rowstride) {
    s16x4 lo = __builtin_amdgcn_ds_read_tr16_b64_v4i16((LAS s16x4*)p);
    s16x4 hi = __builtin_amdgcn_ds_read_tr16_b64_v4i16((LAS s16x4*)(p + 4 * rowstride));
    return __builtin_shufflevector(lo, hi, 0, 1, 2, 3, 4, 5, 6, 7);
}
#define SB() __builtin_amdgcn_sched_barrier(0)
#define EX(x) __builtin_amdgcn_exp2f(x)
#define BC8(x) __builtin_bit_cast(bf16x8, x)

template <bool V> struct BoolC { static constexpr bool value = V; };
#define HX 8
constexpr int L_WP = 0, L_XS = 50240, L_F = L_XS + 4 * 20480, L_YA = L_F + 16384, L_C = L_YA + 14400, L_END = L_C + 256;
enum { C_W = 0, C_Q, C_PK, C_PO, C_PY, C_WQ, C_KV, C_T0, C_T1, C_R0, C_R1, C_R2, C_R3, C_D0, C_D1, C_D2, C_O, C_OS, C_QS, C_OX, C_OY, C_B0, C_B1, C_B2, C_B3, C_B4, C_B5, C_B6, C_X = 36, C_M = 40 };
constexpr int NT0 = 16;

__global__ void NOPK __launch_bounds__(512, 2) fused_kernel(const unsigned short* __restrict__ x, const unsigned short* __restrict__ wqkv,
                                                       const unsigned short* __restrict__ wo, const unsigned short* __restrict__ bx,
                                                       const unsigned short* __restrict__ fwr, const unsigned short* __restrict__ fwi,
                                                       unsigned short* __restrict__ out, char* ws) {
    extern __shared__ __attribute__((aligned(16))) char lds[];
    const int tid = threadIdx.x, lane = tid & 63, wid = __builtin_amdgcn_readfirstlane(tid >> 6);
    const int bid = blockIdx.x, xcd = bid & 7;
    const int hl = (bid >> 3) & (HX - 1), head = (xcd / (HX / 2)) * HX + hl, ti = (xcd & (HX / 2 - 1)) * (32 / HX) + (bid >> 3) / HX;
    const int isv = hl / (HX / 2), pr = (head - hl) / 2 + (hl & (HX / 2 - 1));
    const unsigned lds0 = (unsigned)(uintptr_t)lds;
    const rsrc_t rs = __builtin_amdgcn_make_buffer_rsrc((void*)ws, (short)0, WS_END, 0x00020000);
    LAS unsigned* cnt = (LAS unsigned*)(lds + L_C);
    STAMP_DECL STAMP(0, 0);
    unsigned genv;
    asm volatile("global_load_dword %0, %1, %2 sc1" : "=v"(genv) : "v"(0), "s"(ws) : "memory");
    if (tid < 64) cnt[tid] = 0u;
    BAR();
    const int otile = xcd * 8 + (bid >> 5), cq = (bid >> 3) & 3;
    const int w = wid & 3;
    const int r = lane & 31, h = lane >> 5;
    unsigned tag;
    if (wid < 4) {
        __builtin_amdgcn_s_setprio(3);
        const char* wsrc = (const char*)wqkv + 896 * (1 + isv) + 112 * pr;
        const char* xsrc = (const char*)x + (size_t)(ti * 128 + w * 32 + (lane >> 3)) * 896;
        {
            const int c = w * 64 + lane, row = c / 7;
            int ro = row, co = c - row * 7;
#pragma unroll
            for (int i = 0; i < 13; ++i) {
                if (i < 12 || w == 0) glds16(wsrc + ro * 2688 + co * 16, lds0 + L_WP + (w + 4 * i) * 1024);
                co += 4; ro += 36; if (co >= 7) { co -= 7; ro += 1; }
            }
        }
        auto ldx = [&](int c) __attribute__((always_inline)) {
#pragma unroll
            for (int i = 0; i < 4; ++i)
                glds16(xsrc + i * (8 * 896) + c * 128 + (((lane & 7) ^ ((4 * i + (lane >> 4)) & 7)) * 16), lds0 + L_XS + w * 20480 + (c % 5) * 4096 + i * 1024);
        };
        ldx(0); ldx(1); ldx(2); ldx(3); ldx(4);
        u32x4 xt[8];
#pragma unroll
        for (int i = 0; i < 8; ++i) {
            const char* g = xsrc + (i & 3) * (8 * 896) + (5 + (i >> 2)) * 128 + (((lane & 7) ^ ((4 * (i & 3) + (lane >> 4)) & 7)) * 16);
            xt[i] = *(const u32x4*)g;
        }
        const LAS char* xr = (const LAS char*)lds + L_XS + w * 20480 + r * 128;
        const int xk = (r >> 1) & 7;
        const int trow = 8 * h + ((lane & 15) >> 2), tcol = ((lane >> 4) & 1) * 32 + (lane & 3) * 8;
        const LAS char* wr = (const LAS char*)lds + L_WP + trow * 112 + tcol;
        f32x16 acc0 = f32x16{}, acc1 = f32x16{};
        bf16x8 xf[28], wf[8];
        asm volatile("s_waitcnt vmcnt(28)\n\tv_readfirstlane_b32 %0, %1" : "=s"(tag) : "v"(genv) : "memory"); arrive(cnt + C_W, lane); waitc(cnt + C_W, 4u);
        tag += 1u;
        STAMP(0, 1);
#pragma unroll
        for (int kk = 0; kk < 4; ++kk) { wf[2 * kk] = trfrag(wr + kk * 16 * 112, 112); wf[2 * kk + 1] = trfrag(wr + kk * 16 * 112 + 56, 112); }
        auto gemm = [&](auto isv_c) __attribute__((always_inline)) {
        constexpr bool ISV = decltype(isv_c)::value;
#pragma unroll
        for (int c = 0; c < 7; ++c) {
            if (c == 0) WAITV(24); else if (c == 1) WAITV(20); else if (c == 2) WAITV(16); else if (c == 3) WAITV(12); else if (c == 4) WAITV(8);
            else if (c == 5) {
                arrive(cnt + C_X + w, lane);
#pragma unroll
                for (int i = 0; i < 8; ++i) *(LAS u32x4*)(lds + L_XS + w * 20480 + i * 1024 + lane * 16) = xt[i];
                WAITL();
            }
#pragma unroll
            for (int kk = 0; kk < 4; ++kk) xf[4 * c + kk] = *(const LAS bf16x8*)(xr + (c % 5) * 4096 + (((2 * kk + h) ^ xk) * 16));
#define WNEXT if (c < 6) { wf[2 * kk] = trfrag(wr + ((c + 1) * 64 + kk * 16) * 112, 112); wf[2 * kk + 1] = trfrag(wr + ((c + 1) * 64 + kk * 16) * 112 + 56, 112); } SB();
            SB();
            if (ISV) {
#pragma unroll
                for (int kk = 0; kk < 4; ++kk) { acc0 = MFMA32(xf[4 * c + kk], wf[2 * kk], acc0); acc1 = MFMA32(xf[4 * c + kk], wf[2 * kk + 1], acc1); SB(); WNEXT }
            } else {
#pragma unroll
                for (int kk = 0; kk < 4; ++kk) { acc0 = MFMA32(wf[2 * kk], xf[4 * c + kk], acc0); acc1 = MFMA32(wf[2 * kk + 1], xf[4 * c + kk], acc1); SB(); WNEXT }
            }
        }
        };
        if (isv) gemm(BoolC<true>{}); else gemm(BoolC<false>{});
        STAMP(0, 2);
        {
            const int h0 = 2 * pr, dst = WS_KV + (((h0 * 64 + ti * 4 + w) * 4 + (isv ? 2 : 0)) * 64 + lane) * 16;
#pragma unroll
            for (int j = 0; j < 2; ++j) {
                const f32x16 a = j ? acc1 : acc0;
                u32x4 f0, f1;
#pragma unroll
                for (int e = 0; e < 4; ++e) { f0[e] = cvtpk(a[2 * e], a[2 * e + 1]); f1[e] = cvtpk(a[8 + 2 * e], a[9 + 2 * e]); }
                if (isv) { if (r >= 28) { const unsigned v = r == 28 ? 0x3f803f80u : 0u; f0 = u32x4{v, v, v, v}; f1 = f0; } }
                else if (h) { f1[2] = 0x3f803f80u; f1[3] = 0x00003f80u; }
                st128(rs, dst + j * 262144, f0); st128(rs, dst + j * 262144 + 1024, f1);
            }
        }
        publish(rs, cnt + C_Q, F_KV + (pr * 32 + isv * 16 + ti) * 128, tag, lane, 1);
        STAMP(0, 3);
        waitc(cnt + C_WQ, 4u);
        STAMP(0, 4);
        bf16x8 q0, q1;
        {
            const LAS char* qr = (const LAS char*)lds + L_XS + 8192 + trow * 64 + tcol + ((56 * head) & 15);
            f32x16 qa = f32x16{}, qb = f32x16{};
#pragma unroll
            for (int g = 0; g < 4; ++g) {
                bf16x8 wf[7];
#pragma unroll
                for (int kk = 0; kk < 7; ++kk) {
                    wf[kk] = trfrag(qr + g * 20480 + kk * 1024, 64);
                    const int ks = 7 * g + kk;
                    if (ks >= 16) xf[ks] = *(const LAS bf16x8*)(xr + ((ks >> 2) % 5) * 4096 + (((2 * (ks & 3) + h) ^ xk) * 16));
                }
                SB();
#pragma unroll
                for (int kk = 0; kk < 7; ++kk) { if (kk & 1) qb = MFMA32(wf[kk], xf[7 * g + kk], qb); else qa = MFMA32(wf[kk], xf[7 * g + kk], qa); }
            }
            qa = (qa + qb) * QSCALE;
            u32x4 f0, f1;
#pragma unroll
            for (int e = 0; e < 4; ++e) { f0[e] = cvtpk(qa[2 * e], qa[2 * e + 1]); f1[e] = cvtpk(qa[8 + 2 * e], qa[9 + 2 * e]); }
            if (h) { f1[2] = 0u; f1[3] = 0u; }
            q0 = BC8(f0); q1 = BC8(f1);
        }
        LAS bf16x8* qs_ = (LAS bf16x8*)(lds + L_WP) + lane;
        qs_[w * 128] = q0;
        STAMP(0, 5);
        if (w == 0) { poll32(rs, F_KV + ((head >> 1) * 32 + (lane & 31)) * 128, tag); arrive(cnt + C_PK, lane); }
        else waitc(cnt + C_PK, 1u);
        STAMP(0, 6);
        const int kvo = WS_KV + head * 262144 + lane * 16;
        bf16x8 k0, k1, v0, v1, v1p, n0, n1, n2, n3;
        f32x16 sa, sb, o, oo[4];
        {
            n0 = BC8(ld128(rs, kvo)); n1 = BC8(ld128(rs, kvo + 1024));
            const int t0 = kvo + w * 4096;
            k0 = BC8(ld128(rs, t0)); k1 = BC8(ld128(rs, t0 + 1024)); v0 = BC8(ld128(rs, t0 + 2048)); v1 = BC8(ld128(rs, t0 + 3072));
            sa = MFMA32(n0, q0, f32x16{}); sa = MFMA32(n1, q1, sa);
            n0 = BC8(ld128(rs, t0 + 16384)); n1 = BC8(ld128(rs, t0 + 17408)); n2 = BC8(ld128(rs, t0 + 18432)); n3 = BC8(ld128(rs, t0 + 19456));
            q1 = set_ref(q1, xhalf_max(max16(sa)), h);
            qs_[w * 128 + 64] = q1;
            arrive(cnt + C_QS, lane); waitc(cnt + C_QS, 4u);
        }
        bf16x8 qa[4], qb[4];
#pragma unroll
        for (int i = 0; i < 4; ++i) { qa[i] = qs_[i * 128]; qb[i] = qs_[i * 128 + 64]; oo[i] = f32x16{}; }
        __builtin_amdgcn_s_setprio(0);
        STAMP(0, 7); CSTAMP0;
        sa = MFMA32(k0, qa[0], f32x16{}); sa = MFMA32(k1, qb[0], sa);
        v1p = v1;
        u32x4 p0, p1 = u32x4{};
        float e0 = 0.f, e1 = 0.f, e2 = 0.f, e3 = 0.f;
#define E4(SA, i) e0 = EX(SA[i]); e1 = EX(SA[i + 1]); e2 = EX(SA[i + 2]); e3 = EX(SA[i + 3]); SB();
#define STEPQ(SA, SN, K0, K1, QN, OP, VP, OC, V0) \
        SN = MFMA32(K0, qa[QN], f32x16{}); SB(); p1[2] = cvtpk(e0, e1); p1[3] = cvtpk(e2, e3); SB(); \
        E4(SA, 0) \
        oo[OP] = MFMA32(VP, BC8(p1), oo[OP]); SB(); p0[0] = cvtpk(e0, e1); p0[1] = cvtpk(e2, e3); SB(); \
        E4(SA, 4) \
        SN = MFMA32(K1, qb[QN], SN); SB(); p0[2] = cvtpk(e0, e1); p0[3] = cvtpk(e2, e3); SB(); \
        E4(SA, 8) \
        oo[OC] = MFMA32(V0, BC8(p0), oo[OC]); SB(); p1[0] = cvtpk(e0, e1); p1[1] = cvtpk(e2, e3); SB(); \
        E4(SA, 12)
#define TILE(K0, K1, V0, V1, VP, N0, N1, OFF) \
        STEPQ(sa, sb, K0, K1, 1, 3, VP, 0, V0) \
        VP = BC8(ld128(rs, (OFF) - 16384 + 3072)); SB(); \
        STEPQ(sb, sa, K0, K1, 2, 0, V1, 1, V0) \
        STEPQ(sa, sb, K0, K1, 3, 1, V1, 2, V0) \
        K0 = BC8(ld128(rs, (OFF))); K1 = BC8(ld128(rs, (OFF) + 1024)); SB(); \
        STEPQ(sb, sa, N0, N1, 0, 2, V1, 3, V0) \
        V0 = BC8(ld128(rs, (OFF) + 2048)); SB();
        n3 = v1p;
#pragma unroll 1
        for (int i = 0; i < 8; ++i) {
            const int off = kvo + (((2 * i + 2) * 4 + w) & 63) * 4096;
            const int offb = i == 7 ? off + 64 * 4096 : off;
            STEPQ(sa, sb, k0, k1, 1, 3, n3, 0, v0)
            n3 = BC8(ld128(rs, offb - 16384 + 3072)); SB();
            STEPQ(sb, sa, k0, k1, 2, 0, v1, 1, v0)
            STEPQ(sa, sb, k0, k1, 3, 1, v1, 2, v0)
            k0 = BC8(ld128(rs, off)); k1 = BC8(ld128(rs, off + 1024)); SB();
            STEPQ(sb, sa, n0, n1, 0, 2, v1, 3, v0)
            v0 = BC8(ld128(rs, off + 2048)); SB();
            TILE(n0, n1, n2, n3, v1, k0, k1, off + 16384)
        }
        p1[2] = cvtpk(e0, e1); p1[3] = cvtpk(e2, e3);
        oo[3] = MFMA32(n3, BC8(p1), oo[3]);
        STAMP(0, 8); CSTAMP1;
        __builtin_amdgcn_s_setprio(3);
        {
            LAS f32x4* ox = (LAS f32x4*)(lds + 16384) + lane;
#pragma unroll
            for (int i = 0; i < 4; ++i) {
                if (i != w) {
#pragma unroll
                    for (int e = 0; e < 4; ++e) ox[((w * 4 + i) * 4 + e) * 64] = f32x4{oo[i][4 * e], oo[i][4 * e + 1], oo[i][4 * e + 2], oo[i][4 * e + 3]};
                } else o = oo[i];
            }
            arrive(cnt + C_OX, lane); waitc(cnt + C_OX, 4u);
#pragma unroll
            for (int i = 1; i < 4; ++i) {
#pragma unroll
                for (int e = 0; e < 4; ++e) { const f32x4 v = ox[((((w + i) & 3) * 4 + w) * 4 + e) * 64]; o[4 * e] += v[0]; o[4 * e + 1] += v[1]; o[4 * e + 2] += v[2]; o[4 * e + 3] += v[3]; }
            }
        }
        float l = hi_half(o[12]);
        float chk = 0.f;
#pragma unroll
        for (int e = 0; e < 16; ++e) chk += o[e] * 0.f;
        if (__builtin_amdgcn_ballot_w64(!(l > 0.25f && l < 1.0e18f && chk == 0.f)) != 0ull) {
            STAMP(0, 14);
            const int g = WS_KV + head * 262144 + lane * 16;
            q1 = set_ref(q1, 0.f, h);
            float m = -INFINITY;
#pragma unroll 1
            for (int t = 0; t < 64; ++t) {
                f32x16 s = MFMA32(BC8(ld128(rs, g + t * 4096)), q0, f32x16{});
                s = MFMA32(BC8(ld128(rs, g + t * 4096 + 1024)), q1, s);
                m = __builtin_fmaxf(m, max16(s));
            }
            q1 = set_ref(q1, xhalf_max(m), h);
            o = f32x16{};
#pragma unroll 1
            for (int t = 0; t < 64; ++t) {
                f32x16 s = MFMA32(BC8(ld128(rs, g + t * 4096)), q0, f32x16{});
                s = MFMA32(BC8(ld128(rs, g + t * 4096 + 1024)), q1, s);
#pragma unroll
                for (int e = 0; e < 4; ++e) { p0[e] = cvtpk(EX(s[2 * e]), EX(s[2 * e + 1])); p1[e] = cvtpk(EX(s[8 + 2 * e]), EX(s[9 + 2 * e])); }
                o = MFMA32(BC8(ld128(rs, g + t * 4096 + 2048)), BC8(p0), o);
                o = MFMA32(BC8(ld128(rs, g + t * 4096 + 3072)), BC8(p1), o);
            }
            l = hi_half(o[12]);
        }
        const float inv = 1.0f / l;
#pragma unroll
        for (int e = 0; e < 4; ++e) { p0[e] = cvtpk(o[2 * e] * inv, o[2 * e + 1] * inv); p1[e] = cvtpk(o[8 + 2 * e] * inv, o[9 + 2 * e] * inv); }
        {
            const int dst = WS_OF + ((((ti * 4 + w) * 16 + head) * 2) * 64 + lane) * 16;
            waitc(cnt + C_R0, 4u);
            st128(rs, dst, p0); st128(rs, dst + 1024, p1);
        }
        publish(rs, cnt + C_O, F_O + (ti * 16 + head) * 128, tag, lane, 1);
        STAMP(0, 9);
        STAMP_FLUSH(0);
    } else {
        __builtin_amdgcn_s_setprio(2);
        const int b = bid, t = tid - 256;
        u32x4 wq[7];
        unsigned wv[8];
        LAS cf* fb = (LAS cf*)(lds + L_F);
        LAS cf* f = fb + w * 7 * RSTR;
        const int l2 = lane < 56 ? lane : 55;
        const int n2 = 2 * t, pos = (n2 >> 6) * RSTR + (n2 & 63);
        const float rr = 0.70710678118654752f;
        const int s2 = l2 >> 3, r0 = l2 & 7, m1 = l2 / 7, s3 = l2 - 7 * m1;
        const int j = 8 * w + (lane >> 3), j0 = (lane >> 3) & 3, d0 = 2 * w + (lane >> 5);
        cf a[8], t448[7], t64[8], t256[8], t32[8], tb1, tb2, tb3, tb4;
        {
            unsigned xv[8];
            const int tc = t < 224 ? t : 223;
#pragma unroll
            for (int i = 0; i < 8; ++i) xv[i] = *(const unsigned*)(bx + (size_t)(256 * i + b) * 448 + 2 * tc);
            const int item = bid * 112 + (t < 112 ? t : 111), l = item & 63, fr = item >> 6, hd = fr & 15, cb = fr >> 4;
            const int g = l >> 4, dd = 16 * (g >> 1) + 4 * (g & 1);
            const unsigned short* src = wo + (size_t)(hd * 28) * 448 + cb * 16 + (l & 15);
            if (w < 2) {
#pragma unroll
                for (int e = 0; e < 8; ++e) { const int d = dd + 8 * (e >> 2) + (e & 3); wv[e] = src[(d < 28 ? d : 0) * 448]; if (d >= 28) wv[e] = 0u; }
            }
#pragma unroll
            for (int i = 1; i < 8; ++i) { if (i < 7) t448[i] = tw(lane * i, 1.f / 448); t64[i] = tw(r0 * i, 1.f / 64); t256[i] = tw(j * i, 1.f / 256); t32[i] = tw(j0 * i, 1.f / 32); }
            tb1 = tw(b, 1.f / 2048); tb2 = tw(2 * b, 1.f / 2048); tb3 = tw(3 * b, 1.f / 2048); tb4 = tw(b, 1.f / 512);
#pragma unroll
            for (int i = 1; i < 8; ++i) { if (i < 7) asm volatile("" : "+v"(t448[i])); asm volatile("" : "+v"(t64[i]), "+v"(t256[i]), "+v"(t32[i])); }
            asm volatile("" : "+v"(tb1), "+v"(tb2), "+v"(tb3), "+v"(tb4));
            if (t < 224) {
                cf v[8];
#pragma unroll
                for (int i = 0; i < 8; ++i) v[i] = cf{bflo(xv[i]), bfhi(xv[i])};
                cf p0 = v[0] + v[4], m0 = v[0] - v[4], p1 = v[1] + v[5], n1 = v[1] - v[5];
                cf p2 = v[2] + v[6], m2 = v[2] - v[6], p3 = v[3] + v[7], m3 = v[3] - v[7];
                cf e0 = p0 + p2, e1 = p1 + p3;
                cf z0 = e0 + e1, z4 = e0 - e1;
                cf z2r = p0 - p2, z2i = p3 - p1;
                cf qa = (n1 - m3) * rr, qb = (n1 + m3) * rr;
                cf z1r = m0 + qa, z1i = -m2 - qb, z3r = m0 - qa, z3i = m2 - qb;
                const cf w1 = tb1, w2 = tb2, w3 = tb3;
                cf a0, a1;
                *(LAS f32x4*)(fb + pos) = f32x4{z0.x, z4.x, z0.y, z4.y};
                a0 = cmul<-1>(cf{z1r.x, z1i.x}, w1); a1 = cmul<-1>(cf{z1r.y, z1i.y}, w1);
                *(LAS f32x4*)(fb + 7 * RSTR + pos) = f32x4{a0.x, a0.y, a1.x, a1.y};
                a0 = cmul<-1>(cf{z2r.x, z2i.x}, w2); a1 = cmul<-1>(cf{z2r.y, z2i.y}, w2);
                *(LAS f32x4*)(fb + 14 * RSTR + pos) = f32x4{a0.x, a0.y, a1.x, a1.y};
                a0 = cmul<-1>(cf{z3r.x, z3i.x}, w3); a1 = cmul<-1>(cf{z3r.y, z3i.y}, w3);
                *(LAS f32x4*)(fb + 21 * RSTR + pos) = f32x4{a0.x, a0.y, a1.x, a1.y};
            }
            arrive(cnt + C_B0, lane);
            waitc(cnt + C_B0, 4u);
        }
        STAMP(0, 1);
#pragma unroll
        for (int p = 0; p < 7; ++p) a[p] = f[p * RSTR + lane];
        dft7<-1>(a);
#pragma unroll
        for (int s = 1; s < 7; ++s) a[s] = cmul<-1>(a[s], t448[s]);
#pragma unroll
        for (int s = 0; s < 7; ++s) f[s * RSTR + lane] = a[s];
        WAITL();
#pragma unroll
        for (int i = 0; i < 8; ++i) a[i] = f[s2 * RSTR + 8 * i + r0];
        dft8<-1>(a);
        FENCE();
            {
                const char* qs = (const char*)wqkv + ((56 * head) & ~15) + (size_t)(w * 112 + (lane >> 2)) * 2688 + (lane & 3) * 16;
#pragma unroll
                for (int i = 0; i < 7; ++i) wq[i] = *(const u32x4*)(qs + i * (16 * 2688));
            }
        FENCE();
#pragma unroll
        for (int i = 1; i < 8; ++i) a[i] = cmul<-1>(a[i], t64[i]);
        if (lane < 56) {
#pragma unroll
            for (int i = 0; i < 8; ++i) f[s2 * RSTR + 8 * i + r0] = a[i];
        }
        WAITL();
#pragma unroll
        for (int i = 0; i < 8; ++i) a[i] = f[s3 * RSTR + 8 * m1 + i];
        dft8<-1>(a);
        {
            LAS cf* y = (LAS cf*)(lds + L_YA);
            if (w != 0) {
                if (lane < 56) {
#pragma unroll
                    for (int i = 0; i < 4; ++i) y[w * 225 + lane + 56 * i] = a[i];
#pragma unroll
                    for (int i = 4; i < 8; ++i) y[(8 - w) * 225 + 448 - lane - 56 * i] = cconj(a[i]);
                    if (lane == 0) { y[w * 225 + 224] = a[4]; y[(8 - w) * 225] = cconj(a[0]); }
                }
            } else {
                WAITL();
                if (lane < 56) {
#pragma unroll
                    for (int i = 0; i < 8; ++i) f[lane + 56 * i] = a[i];
                }
                if (lane == 0) f[448] = a[0];
                WAITL();
                const cf w4 = cf{tb4.y, -tb4.x} * 0.5f;
#pragma unroll
                for (int i = 0; i < 4; ++i) {
                    const int k = lane + 64 * i;
                    if (k <= 224) {
                        const cf u = f[k], v = cconj(f[448 - k]);
                        y[k] = (u + v) * 0.5f;
                        y[4 * 225 + k] = cmul<-1>(u - v, w4);
                    }
                }
            }
            arrive(cnt + C_B1, lane); waitc(cnt + C_B1, 4u);
#pragma unroll
            for (int i = 0; i < 4; ++i) { const int e = t + 256 * i; if (e < 900) st128(rs, WS_Y1 + b * (NQ * 8) + e * 16, ((LAS u32x4*)y)[e]); }
            if (t < 112) st128(rs, WS_WO + (bid * 112 + t) * 16, u32x4{wv[0] | (wv[1] << 16), wv[2] | (wv[3] << 16), wv[4] | (wv[5] << 16), wv[6] | (wv[7] << 16)});
        }
        asm volatile("s_waitcnt vmcnt(0)\n\tv_readfirstlane_b32 %0, %1" : "=s"(tag) : "v"(genv) : "memory");
        tag += 1u;
        STAMP(0, 2);
        publish(rs, cnt + C_B3, F_Y1 + bid * 4, tag, lane, 8);
        waitc(cnt + C_X + w, 1u);
#pragma unroll
        for (int i = 0; i < 7; ++i) *(LAS u32x4*)(lds + L_XS + w * 20480 + 8192 + i * 1024 + lane * 16) = wq[i];
        arrive(cnt + C_WQ, lane);
        STAMP(0, 3);
        {
            const int grp = (bid & 7) * 29 + (bid >> 3);
            const bool act = (bid >> 3) < 29 && grp < 225;
            const int col = lane & 7, q = (act ? grp : 0) * 8 + col, c = q / 225, k2 = q - c * 225, cs = c > 4 ? c - 8 : c;
            {
                const int dst = WS_OF + ((((ti * 4 + w) * 16 + head) * 2) * 64 + lane) * 16;
                st128(rs, dst, u32x4{}); st128(rs, dst + 1024, u32x4{});
            }
            unsigned fr_[8], fi_[8];
#pragma unroll
            for (int i = 0; i < 8; ++i) {
                const int d = d0 + 8 * (j0 + 4 * (i >> 2)) + 64 * (i & 3), k1 = (cs + 8 * d) & 2047;
                fr_[i] = fwr[k1 * 225 + k2]; fi_[i] = fwi[k1 * 225 + k2];
            }
            waitc(cnt + C_Q, 4u);
            if (w == 0) {
                poll128(rs, F_Y1 + xcd * FREP + lane * 16, tag);
                arrive(cnt + C_B2, lane);
            } else waitc(cnt + C_B2, 1u);
            STAMP(0, 4);
            LAS cf* mb = fb;
#pragma unroll
            for (int i = 0; i < 8; ++i) a[i] = ld64(rs, WS_Y1 + ((32 * i + j) * NQ + q) * 8);
            if (act) {
                dft8<-1>(a);
                STAMP(0, 5);
#pragma unroll
                for (int i = 1; i < 8; ++i) a[i] = cmul<-1>(a[i], t256[i]);
#pragma unroll
                for (int i = 0; i < 8; ++i) mb[(i * 32 + j) * 8 + col] = a[i];
                arrive(cnt + C_B4, lane); waitc(cnt + C_B4, 4u);
#pragma unroll
                for (int i = 0; i < 8; ++i) a[i] = mb[(d0 * 32 + 4 * i + j0) * 8 + col];
                dft8<-1>(a);
#pragma unroll
                for (int i = 1; i < 8; ++i) a[i] = cmul<-1>(a[i], t32[i]);
#pragma unroll
                for (int i = 0; i < 8; ++i) mb[(d0 * 32 + 4 * i + j0) * 8 + col] = a[i];
                WAITL();
#pragma unroll
                for (int i = 0; i < 8; ++i) a[i] = mb[(d0 * 32 + 4 * (j0 + 4 * (i >> 2)) + (i & 3)) * 8 + col];
                dft4<-1>(a[0], a[1], a[2], a[3]); dft4<-1>(a[4], a[5], a[6], a[7]);
#pragma unroll
                for (int i = 0; i < 8; ++i) a[i] = cmul<1>(a[i], cf{bflo(fi_[i]), bflo(fr_[i])});
                dft4<1>(a[0], a[1], a[2], a[3]); dft4<1>(a[4], a[5], a[6], a[7]);
#pragma unroll
                for (int i = 0; i < 8; ++i) mb[(d0 * 32 + 4 * (j0 + 4 * (i >> 2)) + (i & 3)) * 8 + col] = a[i];
                WAITL();
#pragma unroll
                for (int i = 0; i < 8; ++i) a[i] = mb[(d0 * 32 + 4 * i + j0) * 8 + col];
#pragma unroll
                for (int i = 1; i < 8; ++i) a[i] = cmul<1>(a[i], t32[i]);
                dft8<1>(a);
#pragma unroll
                for (int i = 0; i < 8; ++i) mb[(d0 * 32 + 4 * i + j0) * 8 + col] = a[i];
                arrive(cnt + C_B5, lane); waitc(cnt + C_B5, 4u);
#pragma unroll
                for (int i = 0; i < 8; ++i) a[i] = mb[(i * 32 + j) * 8 + col];
#pragma unroll
                for (int i = 1; i < 8; ++i) a[i] = cmul<1>(a[i], t256[i]);
                dft8<1>(a);
#pragma unroll
                for (int i = 0; i < 8; ++i) st64(rs, WS_Y2 + ((32 * i + j) * NQ + q) * 8, a[i]);
            }
        }
        STAMP(0, 6);
        publish(rs, cnt + C_R0, F_Y2 + bid * 4, tag, lane, 8);
        STAMP(0, 7);
        bf16x8 wa[16], wb[16];
        const int cb0 = cq * 7 + w, cb1 = cq * 7 + (w < 3 ? w + 4 : 6);
#pragma unroll
        for (int i = 0; i < 16; ++i) { wa[i] = BC8(ld128(rs, WS_WO + ((cb0 * 16 + i) * 64 + lane) * 16)); wb[i] = BC8(ld128(rs, WS_WO + ((cb1 * 16 + i) * 64 + lane) * 16)); __builtin_amdgcn_s_sleep(6); FENCE(); }
        waitc(cnt + C_R0, 4u);
        STAMP(0, 8);
        if (w == 0) {
            poll128(rs, F_Y2 + xcd * FREP + lane * 16, tag);
            arrive(cnt + C_PY, lane);
            if (bid == 0 && lane == 0) st32(rs, F_GEN, tag);
        } else waitc(cnt + C_PY, 1u);
        {
            const int y = WS_Y2 + b * (NQ * 8);
            cf z[8];
            const int ya = y + (w * 225) * 8, yb = y + ((w ? 8 - w : 0) * 225) * 8;
#pragma unroll
            for (int i = 0; i < 8; ++i) a[i] = ld64(rs, i < 4 ? ya + (l2 + 56 * i) * 8 : yb + (448 - l2 - 56 * i) * 8);
            if (w) { z[0] = ld64(rs, yb); z[4] = ld64(rs, ya + 224 * 8); }
            else {
#pragma unroll
                for (int i = 0; i < 8; ++i) z[i] = ld64(rs, y + (900 + (i < 4 ? l2 + 56 * i : 448 - l2 - 56 * i)) * 8);
            }
            waitc(cnt + C_OX, 1u);
            STAMP(0, 9);
#pragma unroll
            for (int i = 4; i < 8; ++i) a[i] = cconj(a[i]);
            if (w) { if (lane == 0) { a[0] = (a[0] + cconj(z[0])) * 0.5f; a[4] = (a[4] + z[4]) * 0.5f; } }
            else {
                const cf w4 = tb4;
#pragma unroll
                for (int i = 0; i < 8; ++i) {
                    cf z4 = cmul<1>(z[i], w4);
                    if (i >= 4) z4 = cconj(z4);
                    if ((i & 3) == 0 && lane == 0) { a[i].y = 0.f; z4.y = 0.f; }
                    a[i] = addrot<1>(a[i], z4);
                }
            }
        }
        dft8<1>(a);
        if (lane < 56) {
#pragma unroll
            for (int i = 0; i < 8; ++i) f[s3 * RSTR + 8 * m1 + i] = a[i];
        }
        WAITL();
#pragma unroll
        for (int i = 0; i < 8; ++i) a[i] = f[s2 * RSTR + 8 * i + r0];
#pragma unroll
        for (int i = 1; i < 8; ++i) a[i] = cmul<1>(a[i], t64[i]);
        dft8<1>(a);
        if (lane < 56) {
#pragma unroll
            for (int i = 0; i < 8; ++i) f[s2 * RSTR + 8 * i + r0] = a[i];
        }
        WAITL();
#pragma unroll
        for (int s = 0; s < 7; ++s) a[s] = f[s * RSTR + lane];
#pragma unroll
        for (int s = 1; s < 7; ++s) a[s] = cmul<1>(a[s], t448[s]);
        dft7<1>(a);
#pragma unroll
        for (int p = 0; p < 7; ++p) f[p * RSTR + lane] = a[p];
        arrive(cnt + C_B6, lane); waitc(cnt + C_B6, 4u);
        if (t < 224) {
            const float sc = 1.0f / (2048.0f * 448.0f), sc2 = 2.f * sc;
            const f32x4 u0 = *(LAS f32x4*)(fb + pos), u1 = *(LAS f32x4*)(fb + 7 * RSTR + pos), u2 = *(LAS f32x4*)(fb + 14 * RSTR + pos), u3 = *(LAS f32x4*)(fb + 21 * RSTR + pos);
            const cf w1 = tb1 * sc2, w2 = tb2 * sc2, w3 = tb3 * sc2;
            const cf z0 = cf{u0[0], u0[2]} * sc, z4 = cf{u0[1], u0[3]} * sc;
            cf a0 = cmul<1>(cf{u1[0], u1[1]}, w1), a1 = cmul<1>(cf{u1[2], u1[3]}, w1);
            const cf z1r = cf{a0.x, a1.x}, z1i = cf{a0.y, a1.y};
            a0 = cmul<1>(cf{u2[0], u2[1]}, w2); a1 = cmul<1>(cf{u2[2], u2[3]}, w2);
            const cf z2r = cf{a0.x, a1.x}, z2i = cf{a0.y, a1.y};
            a0 = cmul<1>(cf{u3[0], u3[1]}, w3); a1 = cmul<1>(cf{u3[2], u3[3]}, w3);
            const cf z3r = cf{a0.x, a1.x}, z3i = cf{a0.y, a1.y};
            const cf e0 = z0 + z4, e1 = z0 - z4;
            const cf p0 = e0 + z2r, p2 = e0 - z2r, p1 = e1 - z2i, p3 = e1 + z2i;
            const cf m0 = z1r + z3r, m2 = z3i - z1i;
            const cf qa = (z1r - z3r) * rr, qb = (z1i + z3i) * rr;
            const cf mm1 = qa - qb, m3 = -qa - qb;
            const cf yv[8] = {p0 + m0, p1 + mm1, p2 + m2, p3 + m3, p0 - m0, p1 - mm1, p2 - m2, p3 - m3};
#pragma unroll
            for (int i = 0; i < 8; ++i) *(unsigned*)(out + (size_t)(256 * i + b) * 896 + 448 + n2) = cvtpk(yv[i].x, yv[i].y);
        }
        STAMP(0, 10);
        waitc(cnt + C_OX, 4u);
        __builtin_amdgcn_s_setprio(3);
        if (w == 0) { poll32(rs, F_O + ((otile >> 2) * 16 + (lane & 15)) * 128, tag); arrive(cnt + C_PO, lane); }
        else waitc(cnt + C_PO, 1u);
        STAMP(0, 11);
        u32x4 ov[8];
#pragma unroll
        for (int i = 0; i < 8; ++i) ov[i] = ld128(rs, WS_OF + (otile * 2048 + (i * 4 + w) * 64 + lane) * 16);
#pragma unroll
        for (int i = 0; i < 8; ++i) ((LAS u32x4*)(lds + 98304))[(i * 4 + w) * 64 + lane] = ov[i];
        arrive(cnt + C_OS, lane); waitc(cnt + C_OS, 4u);
        STAMP(0, 12);
        {
            const int g = lane >> 4;
            const LAS bf16x8* op = (const LAS bf16x8*)(lds + 98304) + (g >> 1) * 64 + (g & 1) * 32 + (lane & 15);
            f32x4 c0 = f32x4{}, c1 = f32x4{}, c2 = f32x4{}, c3 = f32x4{};
#pragma unroll
            for (int i = 0; i < 16; ++i) {
                const bf16x8 a0 = op[i * 128], a1 = op[i * 128 + 16];
                c0 = MFMA16(wa[i], a0, c0); c1 = MFMA16(wa[i], a1, c1); c2 = MFMA16(wb[i], a0, c2); c3 = MFMA16(wb[i], a1, c3);
            }
            unsigned short* dst = out + (size_t)(otile * 32 + (lane & 15)) * 896 + g * 4;
            *(u32x2*)(dst + cb0 * 16) = u32x2{cvtpk(c0[0], c0[1]), cvtpk(c0[2], c0[3])};
            *(u32x2*)(dst + cb0 * 16 + 16 * 896) = u32x2{cvtpk(c1[0], c1[1]), cvtpk(c1[2], c1[3])};
            if (w < 3) {
                *(u32x2*)(dst + cb1 * 16) = u32x2{cvtpk(c2[0], c2[1]), cvtpk(c2[2], c2[3])};
                *(u32x2*)(dst + cb1 * 16 + 16 * 896) = u32x2{cvtpk(c3[0], c3[1]), cvtpk(c3[2], c3[3])};
            }
        }
        STAMP(0, 13); STAMP_FLUSH(0);
    }
}

extern "C" void kernel_launch(void* const* d_in, const int* in_sizes, int n_in, void* d_out, int out_size, void* d_ws,
                              size_t ws_size, hipStream_t stream) {
    (void)hipFuncSetAttribute((const void*)fused_kernel, hipFuncAttributeMaxDynamicSharedMemorySize, L_END);
    fused_kernel<<<256, 512, L_END, stream>>>((const unsigned short*)d_in[0], (const unsigned short*)d_in[1], (const unsigned short*)d_in[2],
                                              (const unsigned short*)d_in[3], (const unsigned short*)d_in[4], (const unsigned short*)d_in[5],
                                              (unsigned short*)d_out, (char*)d_ws);
}
```

```cpp
#include <hip/hip_runtime.h>
#include <hip/hip_bf16.h>
#include <stdint.h>

typedef __attribute__((ext_vector_type(8))) short bf16x8;
typedef __attribute__((ext_vector_type(4))) short s16x4;
typedef __attribute__((ext_vector_type(16))) float f32x16;
typedef __attribute__((ext_vector_type(4))) float f32x4;
typedef __attribute__((ext_vector_type(4))) unsigned u32x4;
typedef __attribute__((ext_vector_type(2))) unsigned u32x2;
typedef __attribute__((ext_vector_type(2))) float cf;
#define LAS __attribute__((address_space(3)))
#define NOPK __attribute__((target("no-packed-fp32-ops")))
#define DEVI __device__ __forceinline__
#define MFMA32(a, b, c) __builtin_amdgcn_mfma_f32_32x32x16_bf16(a, b, c, 0, 0, 0)
#define MFMA16(a, b, c) __builtin_amdgcn_mfma_f32_16x16x32_bf16(a, b, c, 0, 0, 0)

constexpr int NQ = 1800;
constexpr int F_GEN = 0, F_KV = 256, F_O = F_KV + 256 * 128, F_Y1 = F_O + 256 * 128, FREP = 1024 + 256, F_Y2 = F_Y1 + 8 * FREP;
constexpr int WS_KV = 131072;
constexpr int WS_WO = WS_KV + 16 * 64 * 4 * 1024;
constexpr int WS_OF = WS_WO + 28 * 16 * 1024;
constexpr int WS_Y1 = WS_OF + 64 * 16 * 2 * 1024;
constexpr int WS_Y2 = WS_Y1;
constexpr int WS_END = WS_Y1 + 256 * NQ * 8;
constexpr float QSCALE = 0.18898223650461362f * 1.4426950408889634f;

typedef __attribute__((ext_vector_type(2))) __bf16 bf16x2_t;
DEVI unsigned cvtpk(float lo, float hi) { return __builtin_bit_cast(unsigned, __builtin_convertvector(cf{lo, hi}, bf16x2_t)); }
DEVI float bflo(unsigned u) { return __uint_as_float(u << 16); }
DEVI float bfhi(unsigned u) { return __uint_as_float(u & 0xffff0000u); }
DEVI void glds16(const void* g, unsigned lds_base) {
    unsigned sv;
    asm volatile("s_mov_b32 %0, m0\n\ts_mov_b32 m0, %2\n\ts_nop 0\n\tglobal_load_lds_dwordx4 %1, off\n\ts_mov_b32 m0, %0"
                 : "=&s"(sv) : "v"(g), "s"(__builtin_amdgcn_readfirstlane(lds_base)) : "memory");
}
#define WAITV(N) asm volatile("s_waitcnt vmcnt(" #N ")" ::: "memory")
#define WAITL() asm volatile("s_waitcnt lgkmcnt(0)" ::: "memory")
#define BAR() asm volatile("s_waitcnt lgkmcnt(0)\n\ts_barrier" ::: "memory")

#define STAMP(K, I)
#define STAMP_DECL
#define STAMP_FLUSH(K)
template <int DIR> DEVI cf cmul(cf a, cf w) {
    return DIR > 0 ? cf{__builtin_fmaf(a.x, w.y, -a.y * w.x), __builtin_fmaf(a.y, w.y, a.x * w.x)}
                   : cf{__builtin_fmaf(a.x, w.y, a.y * w.x), __builtin_fmaf(a.y, w.y, -a.x * w.x)};
}
template <int DIR> DEVI cf addrot(cf t, cf x) { return DIR > 0 ? cf{t.x - x.y, t.y + x.x} : cf{t.x + x.y, t.y - x.x}; }
DEVI cf cconj(cf a) { return cf{a.x, -a.y}; }
DEVI cf tw(int num, float inv_den) {
    const float x = (float)num * inv_den;
    return cf{__builtin_amdgcn_sinf(x), __builtin_amdgcn_cosf(x)};
}
template <int DIR> DEVI void dft4(cf& a0, cf& a1, cf& a2, cf& a3) {
    cf t0 = a0 + a2, t1 = a0 - a2, t2 = a1 + a3, t3 = a1 - a3;
    a0 = t0 + t2; a2 = t0 - t2; a1 = addrot<DIR>(t1, t3); a3 = addrot<-DIR>(t1, t3);
}
template <int DIR> DEVI void dft8(cf* a) {
    cf e0 = a[0], e1 = a[2], e2 = a[4], e3 = a[6], o0 = a[1], o1 = a[3], o2 = a[5], o3 = a[7];
    dft4<DIR>(e0, e1, e2, e3); dft4<DIR>(o0, o1, o2, o3);
    const float r = 0.70710678118654752f;
    o1 = addrot<DIR>(o1, o1); o3 = addrot<-DIR>(o3, o3);
    a[0] = e0 + o0; a[4] = e0 - o0; a[1] = e1 + o1 * r; a[5] = e1 - o1 * r;
    a[2] = addrot<DIR>(e2, o2); a[6] = addrot<-DIR>(e2, o2); a[3] = e3 - o3 * r; a[7] = e3 + o3 * r;
}
template <int DIR> DEVI void dft7(cf* a) {
    const float c1 = 0.62348980185873353f, c2 = -0.22252093395631440f, c3 = -0.90096886790241913f;
    const float s1 = 0.78183148246802981f, s2 = 0.97492791218182361f, s3 = 0.43388373911755812f;
    cf t1 = a[1] + a[6], t2 = a[2] + a[5], t3 = a[3] + a[4], u1 = a[1] - a[6], u2 = a[2] - a[5], u3 = a[3] - a[4];
    cf A1 = a[0] + t1 * c1 + t2 * c2 + t3 * c3, A2 = a[0] + t1 * c2 + t2 * c3 + t3 * c1, A3 = a[0] + t1 * c3 + t2 * c1 + t3 * c2;
    cf B1 = u1 * s1 + u2 * s2 + u3 * s3, B2 = u1 * s2 - u2 * s3 - u3 * s1, B3 = u1 * s3 - u2 * s1 + u3 * s2;
    a[0] = a[0] + t1 + t2 + t3;
    a[1] = addrot<DIR>(A1, B1); a[6] = addrot<-DIR>(A1, B1); a[2] = addrot<DIR>(A2, B2); a[5] = addrot<-DIR>(A2, B2);
    a[3] = addrot<DIR>(A3, B3); a[4] = addrot<-DIR>(A3, B3);
}
constexpr int RSTR = 72;
constexpr int RFB = 7 * RSTR * 8;

#define CSTAMP0
#define CSTAMP1
typedef __amdgpu_buffer_rsrc_t rsrc_t;
DEVI void st128(rsrc_t r, int off, u32x4 v) { __builtin_amdgcn_raw_buffer_store_b128(v, r, off, 0, 16); }
DEVI void st64(rsrc_t r, int off, cf v) { __builtin_amdgcn_raw_buffer_store_b64(__builtin_bit_cast(u32x2, v), r, off, 0, 16); }
DEVI void st32(rsrc_t r, int off, unsigned v) { __builtin_amdgcn_raw_buffer_store_b32(v, r, off, 0, 16); }
DEVI u32x4 ld128(rsrc_t r, int off) { return __builtin_amdgcn_raw_buffer_load_b128(r, off, 0, 16); }
DEVI cf ld64(rsrc_t r, int off) { return __builtin_bit_cast(cf, __builtin_amdgcn_raw_buffer_load_b64(r, off, 0, 16)); }
DEVI unsigned ld32(rsrc_t r, int off) { return __builtin_amdgcn_raw_buffer_load_b32(r, off, 0, 16); }
DEVI void glds16c(const void* g, unsigned lds_base) {
    unsigned sv;
    asm volatile("s_mov_b32 %0, m0\n\ts_mov_b32 m0, %2\n\ts_nop 0\n\tglobal_load_lds_dwordx4 %1, off sc1\n\ts_mov_b32 m0, %0"
                 : "=&s"(sv) : "v"(g), "s"(__builtin_amdgcn_readfirstlane(lds_base)) : "memory");
}
#define FENCE() asm volatile("" ::: "memory")
constexpr int SPIN_CAP = 1 << 18;
DEVI void arrive(LAS unsigned* c, int) {
    asm volatile("s_waitcnt lgkmcnt(0)\n\ts_mov_b64 exec, 1\n\tds_add_u32 %0, %1\n\ts_mov_b64 exec, -1" :: "v"(c), "v"(1u) : "memory");
}
DEVI unsigned arrive_rtn(LAS unsigned* c) {
    unsigned v, r;
    asm volatile("s_waitcnt lgkmcnt(0)\n\ts_mov_b64 exec, 1\n\tds_add_rtn_u32 %0, %2, %3\n\ts_waitcnt lgkmcnt(0)\n\tv_readfirstlane_b32 %1, %0\n\ts_mov_b64 exec, -1"
                 : "=&v"(v), "=s"(r) : "v"(c), "v"(1u) : "memory");
    return r;
}
DEVI void waitc(LAS unsigned* c, unsigned n) {
    unsigned v, sv, it;
    asm volatile("s_mov_b32 %2, 0x200000\n"
                 "1:\n\tds_read_b32 %0, %3\n\ts_waitcnt lgkmcnt(0)\n\tv_readfirstlane_b32 %1, %0\n\ts_cmp_ge_u32 %1, %4\n\ts_cbranch_scc1 2f\n\t"
                 "s_sleep 3\n\ts_sub_u32 %2, %2, 1\n\ts_cbranch_scc0 1b\n2:"
                 : "=&v"(v), "=&s"(sv), "=&s"(it) : "v"(c), "s"(n) : "memory", "scc");
}
DEVI void publish(rsrc_t r, LAS unsigned* c, int flag_off, unsigned tag, int lane, int nrep) {
    WAITV(0);
    if (arrive_rtn(c) == 3u && lane < nrep) st32(r, flag_off + lane * FREP, tag);
}
DEVI void poll32(rsrc_t r, int off, unsigned tag) {
#pragma unroll 1
    for (int i = 0; i < SPIN_CAP; ++i) { if (__builtin_amdgcn_ballot_w64(ld32(r, off) != tag) == 0ull) break; __builtin_amdgcn_s_sleep(2); FENCE(); }
    FENCE();
}
DEVI void poll128(rsrc_t r, int off, unsigned tag) {
#pragma unroll 1
    for (int i = 0; i < SPIN_CAP; ++i) {
        const u32x4 v = ld128(r, off);
        if (__builtin_amdgcn_ballot_w64(!(v[0] == tag && v[1] == tag && v[2] == tag && v[3] == tag)) == 0ull) break;
        __builtin_amdgcn_s_sleep(10); FENCE();
    }
    FENCE();
}

DEVI float xhalf_max(float m) {
    auto rr = __builtin_amdgcn_permlane32_swap(__float_as_uint(m), __float_as_uint(m), false, false);
    return __builtin_fmaxf(__uint_as_float(rr[0]), __uint_as_float(rr[1]));
}
DEVI float hi_half(float m) {
    auto rr = __builtin_amdgcn_permlane32_swap(__float_as_uint(m), __float_as_uint(m), false, false);
    return __uint_as_float(rr[1]);
}
DEVI float max16(const f32x16& s) {
    float m = __builtin_fmaxf(s[0], s[1]);
#pragma unroll
    for (int i = 2; i < 16; ++i) m = __builtin_fmaxf(m, s[i]);
    return m;
}
DEVI bf16x8 set_ref(bf16x8 q1, float m, int h) {
    const float nm = -m;
    const unsigned b0 = __float_as_uint(nm) & 0xffff0000u; const float r1 = nm - __uint_as_float(b0);
    const unsigned b1 = __float_as_uint(r1) & 0xffff0000u; const float r2 = r1 - __uint_as_float(b1);
    const unsigned b2 = __float_as_uint(r2) & 0xffff0000u;
    u32x4 u = __builtin_bit_cast(u32x4, q1);
    if (h) { u[2] = (b0 >> 16) | b1; u[3] = b2 >> 16; }
    return __builtin_bit_cast(bf16x8, u);
}
DEVI bf16x8 trfrag(const LAS char* p, int rowstride) {
    s16x4 lo = __builtin_amdgcn_ds_read_tr16_b64_v4i16((LAS s16x4*)p);
    s16x4 hi = __builtin_amdgcn_ds_read_tr16_b64_v4i16((LAS s16x4*)(p + 4 * rowstride));
    return __builtin_shufflevector(lo, hi, 0, 1, 2, 3, 4, 5, 6, 7);
}
#define SB() __builtin_amdgcn_sched_barrier(0)
#define EX(x) __builtin_amdgcn_exp2f(x)
#define BC8(x) __builtin_bit_cast(bf16x8, x)

#define HX 2
constexpr int L_WP = 0, L_XS = 50240, L_F = L_XS + 4 * 20480, L_YA = L_F + 16384, L_C = L_YA + 14400, L_END = L_C + 256;
enum { C_W = 0, C_Q, C_PK, C_PO, C_PY, C_WQ, C_KV, C_T0, C_T1, C_R0, C_R1, C_R2, C_R3, C_D0, C_D1, C_D2, C_O, C_OS, C_QS, C_OX, C_OY, C_B0, C_B1, C_B2, C_B3, C_B4, C_B5, C_B6, C_X = 36, C_M = 40 };
constexpr int NT0 = 16;

__global__ void NOPK __launch_bounds__(512, 2) fused_kernel(const unsigned short* __restrict__ x, const unsigned short* __restrict__ wqkv,
                                                       const unsigned short* __restrict__ wo, const unsigned short* __restrict__ bx,
                                                       const unsigned short* __restrict__ fwr, const unsigned short* __restrict__ fwi,
                                                       unsigned short* __restrict__ out, char* ws) {
    extern __shared__ __attribute__((aligned(16))) char lds[];
    const int tid = threadIdx.x, lane = tid & 63, wid = __builtin_amdgcn_readfirstlane(tid >> 6);
    const int bid = blockIdx.x, xcd = bid & 7;
    const int hl = (bid >> 3) & (HX - 1), head = (xcd / (HX / 2)) * HX + hl, ti = (xcd & (HX / 2 - 1)) * (32 / HX) + (bid >> 3) / HX;
    const int isv = hl / (HX / 2), pr = (head - hl) / 2 + (hl & (HX / 2 - 1));
    const unsigned lds0 = (unsigned)(uintptr_t)lds;
    const rsrc_t rs = __builtin_amdgcn_make_buffer_rsrc((void*)ws, (short)0, WS_END, 0x00020000);
    LAS unsigned* cnt = (LAS unsigned*)(lds + L_C);
    STAMP_DECL STAMP(0, 0);
    unsigned genv;
    asm volatile("global_load_dword %0, %1, %2 sc1" : "=v"(genv) : "v"(0), "s"(ws) : "memory");
    if (tid < 64) cnt[tid] = 0u;
    BAR();
    const int otile = xcd * 8 + (bid >> 5), cq = (bid >> 3) & 3;
    const int w = wid & 3;
    const int r = lane & 31, h = lane >> 5;
    unsigned tag;
    if (wid < 4) {
        __builtin_amdgcn_s_setprio(3);
        const char* xsrc = (const char*)x + (size_t)(ti * 128 + w * 32 + (lane >> 3)) * 896;
        u32x4 xt[8];
#pragma unroll
        for (int i = 0; i < 8; ++i) {
            const char* g = xsrc + (i & 3) * (8 * 896) + (5 + (i >> 2)) * 128 + (((lane & 7) ^ ((4 * (i & 3) + (lane >> 4)) & 7)) * 16);
            xt[i] = *(const u32x4*)g;
        }
        const LAS char* xr = (const LAS char*)lds + L_XS + w * 20480 + r * 128;
        const int xk = (r >> 1) & 7;
        const int trow = 8 * h + ((lane & 15) >> 2), tcol = ((lane >> 4) & 1) * 32 + (lane & 3) * 8;
        const LAS char* wr = (const LAS char*)lds + L_WP + trow * 112 + tcol;
        f32x16 acc0 = f32x16{}, acc1 = f32x16{};
        bf16x8 xf[28], wf[8];
        u32x4 wq[7];
        const char* qs = (const char*)wqkv + ((56 * head) & ~15) + (size_t)(w * 112 + (lane >> 2)) * 2688 + (lane & 3) * 16;
        asm volatile("s_waitcnt vmcnt(8)\n\tv_readfirstlane_b32 %0, %1" : "=s"(tag) : "v"(genv) : "memory"); waitc(cnt + C_M + 1, 4u);
        tag += 1u;
        STAMP(0, 1);
#pragma unroll
        for (int kk = 0; kk < 4; ++kk) { wf[2 * kk] = trfrag(wr + kk * 16 * 112, 112); wf[2 * kk + 1] = trfrag(wr + kk * 16 * 112 + 56, 112); }
#pragma unroll
        for (int c = 0; c < 7; ++c) {
#define XRD(C) _Pragma("unroll") for (int kk = 0; kk < 4; ++kk) xf[4 * (C) + kk] = *(const LAS bf16x8*)(xr + ((C) % 5) * 4096 + (((2 * kk + h) ^ xk) * 16));
            if (c == 0) XRD(0)
            if (c == 1) waitc(cnt + C_M + 3, 4u); else if (c == 3) waitc(cnt + C_M + 6, 4u);
            if (c == 3) {
#pragma unroll
                for (int i = 0; i < 8; ++i) *(LAS u32x4*)(lds + L_XS + w * 20480 + i * 1024 + lane * 16) = xt[i];
                FENCE();
            }
            if (c < 6) XRD(c + 1)
            if (c >= 1 && c < 5) { wq[2 * c - 2] = *(const u32x4*)(qs + (2 * c - 2) * (16 * 2688)); if (c < 4) wq[2 * c - 1] = *(const u32x4*)(qs + (2 * c - 1) * (16 * 2688)); }
#define WNEXT if (c < 6) { wf[2 * kk] = trfrag(wr + ((c + 1) * 64 + kk * 16) * 112, 112); wf[2 * kk + 1] = trfrag(wr + ((c + 1) * 64 + kk * 16) * 112 + 56, 112); } SB();
            SB();
            {
#pragma unroll
                for (int kk = 0; kk < 4; ++kk) { acc0 = MFMA32(wf[2 * kk], xf[4 * c + kk], acc0); acc1 = MFMA32(wf[2 * kk + 1], xf[4 * c + kk], acc1); SB(); WNEXT }
            }
        }
        STAMP(0, 2);
        {
            const int h0 = 2 * pr, dst = WS_KV + (((h0 * 64 + ti * 4 + w) * 4 + (isv ? 2 : 0)) * 64 + lane) * 16;
#pragma unroll
            for (int j = 0; j < 2; ++j) {
                const f32x16 a = j ? acc1 : acc0;
                u32x4 f0, f1;
#pragma unroll
                for (int e = 0; e < 4; ++e) { f0[e] = cvtpk(a[2 * e], a[2 * e + 1]); f1[e] = cvtpk(a[8 + 2 * e], a[9 + 2 * e]); }
                if (h) { f1[2] = isv ? 0x00003f80u : 0x3f803f80u; f1[3] = isv ? 0u : 0x00003f80u; }
                if (isv) {
                    LAS char* tb = (LAS char*)lds + L_XS + w * 20480 + 8192 + j * 2048;
                    LAS u32x2* tw_ = (LAS u32x2*)(tb + r * 64 + h * 8);
                    tw_[0] = u32x2{f0[0], f0[1]}; tw_[2] = u32x2{f0[2], f0[3]}; tw_[4] = u32x2{f1[0], f1[1]}; tw_[6] = u32x2{f1[2], f1[3]};
                    WAITL();
                    const LAS char* tr_ = tb + (4 * h + ((lane & 15) >> 2)) * 64 + tcol;
                    f0 = __builtin_bit_cast(u32x4, trfrag(tr_, 128)); f1 = __builtin_bit_cast(u32x4, trfrag(tr_ + 1024, 128));
                    WAITL();
                }
                st128(rs, dst + j * 262144, f0); st128(rs, dst + j * 262144 + 1024, f1);
            }
        }
#pragma unroll
        for (int i = 0; i < 7; ++i) *(LAS u32x4*)(lds + L_XS + w * 20480 + 8192 + i * 1024 + lane * 16) = wq[i];
        arrive(cnt + C_WQ, lane); waitc(cnt + C_WQ, 4u);
        STAMP(0, 3);
        STAMP(0, 4);
        bf16x8 q0, q1;
        {
            const LAS char* qr = (const LAS char*)lds + L_XS + 8192 + trow * 64 + tcol + ((56 * head) & 15);
            f32x16 qa = f32x16{}, qb = f32x16{};
#pragma unroll
            for (int g = 0; g < 4; ++g) {
                if (g == 2) publish(rs, cnt + C_Q, F_KV + (pr * 32 + isv * 16 + ti) * 128, tag, lane, 1);
                bf16x8 wf[7];
#pragma unroll
                for (int kk = 0; kk < 7; ++kk) {
                    wf[kk] = trfrag(qr + g * 20480 + kk * 1024, 64);
                    const int ks = 7 * g + kk;
                    if (ks >= 16) xf[ks] = *(const LAS bf16x8*)(xr + ((ks >> 2) % 5) * 4096 + (((2 * (ks & 3) + h) ^ xk) * 16));
                }
                SB();
#pragma unroll
                for (int kk = 0; kk < 7; ++kk) { if (kk & 1) qb = MFMA32(wf[kk], xf[7 * g + kk], qb); else qa = MFMA32(wf[kk], xf[7 * g + kk], qa); }
            }
            qa = (qa + qb) * QSCALE;
            u32x4 f0, f1;
#pragma unroll
            for (int e = 0; e < 4; ++e) { f0[e] = cvtpk(qa[2 * e], qa[2 * e + 1]); f1[e] = cvtpk(qa[8 + 2 * e], qa[9 + 2 * e]); }
            if (h) { f1[2] = 0u; f1[3] = 0u; }
            q0 = BC8(f0); q1 = BC8(f1);
        }
        LAS bf16x8* qs_ = (LAS bf16x8*)(lds + L_WP) + lane;
        qs_[w * 128] = q0;
        STAMP(0, 5);
        if (w == 0) { poll32(rs, F_KV + ((head >> 1) * 32 + (lane & 31)) * 128, tag); arrive(cnt + C_PK, lane); }
        else waitc(cnt + C_PK, 1u);
        STAMP(0, 6);
        const int kvo = WS_KV + head * 262144 + lane * 16;
        bf16x8 k0, k1, v0, v1, v1p, n0, n1, n2, n3;
        f32x16 sa, sb, o, oo[4];
        {
            n0 = BC8(ld128(rs, kvo)); n1 = BC8(ld128(rs, kvo + 1024));
            const int t0 = kvo + w * 4096;
            k0 = BC8(ld128(rs, t0)); k1 = BC8(ld128(rs, t0 + 1024)); v0 = BC8(ld128(rs, t0 + 2048)); v1 = BC8(ld128(rs, t0 + 3072));
            sa = MFMA32(n0, q0, f32x16{}); sa = MFMA32(n1, q1, sa);
            n0 = BC8(ld128(rs, t0 + 16384)); n1 = BC8(ld128(rs, t0 + 17408)); n2 = BC8(ld128(rs, t0 + 18432)); n3 = BC8(ld128(rs, t0 + 19456));
            q1 = set_ref(q1, xhalf_max(max16(sa)), h);
            qs_[w * 128 + 64] = q1;
            arrive(cnt + C_QS, lane); waitc(cnt + C_QS, 4u);
        }
        bf16x8 qa[4], qb[4];
#pragma unroll
        for (int i = 0; i < 4; ++i) { qa[i] = qs_[i * 128]; qb[i] = qs_[i * 128 + 64]; oo[i] = f32x16{}; }
        __builtin_amdgcn_s_setprio(0);
        STAMP(0, 7); CSTAMP0;
        sa = MFMA32(k0, qa[0], f32x16{}); sa = MFMA32(k1, qb[0], sa);
        v1p = v1;
        u32x4 p0, p1 = u32x4{};
        float e0 = 0.f, e1 = 0.f, e2 = 0.f, e3 = 0.f;
#define E4(SA, i) e0 = EX(SA[i]); e1 = EX(SA[i + 1]); e2 = EX(SA[i + 2]); e3 = EX(SA[i + 3]); SB();
#define STEPQ(SA, SN, K0, K1, QN, OP, VP, OC, V0) \
        SN = MFMA32(K0, qa[QN], f32x16{}); SB(); p1[2] = cvtpk(e0, e1); p1[3] = cvtpk(e2, e3); SB(); \
        E4(SA, 0) \
        oo[OP] = MFMA32(VP, BC8(p1), oo[OP]); SB(); p0[0] = cvtpk(e0, e1); p0[1] = cvtpk(e2, e3); SB(); \
        E4(SA, 4) \
        SN = MFMA32(K1, qb[QN], SN); SB(); p0[2] = cvtpk(e0, e1); p0[3] = cvtpk(e2, e3); SB(); \
        E4(SA, 8) \
        oo[OC] = MFMA32(V0, BC8(p0), oo[OC]); SB(); p1[0] = cvtpk(e0, e1); p1[1] = cvtpk(e2, e3); SB(); \
        E4(SA, 12)
#define TILE(K0, K1, V0, V1, VP, N0, N1, OFF) \
        STEPQ(sa, sb, K0, K1, 1, 3, VP, 0, V0) \
        VP = BC8(ld128(rs, (OFF) - 16384 + 3072)); SB(); \
        STEPQ(sb, sa, K0, K1, 2, 0, V1, 1, V0) \
        STEPQ(sa, sb, K0, K1, 3, 1, V1, 2, V0) \
        K0 = BC8(ld128(rs, (OFF))); K1 = BC8(ld128(rs, (OFF) + 1024)); SB(); \
        STEPQ(sb, sa, N0, N1, 0, 2, V1, 3, V0) \
        V0 = BC8(ld128(rs, (OFF) + 2048)); SB();
        n3 = v1p;
#pragma unroll 1
        for (int i = 0; i < 8; ++i) {
            const int off = kvo + (((2 * i + 2) * 4 + w) & 63) * 4096;
            const int offb = i == 7 ? off + 64 * 4096 : off;
            STEPQ(sa, sb, k0, k1, 1, 3, n3, 0, v0)
            n3 = BC8(ld128(rs, offb - 16384 + 3072)); SB();
            STEPQ(sb, sa, k0, k1, 2, 0, v1, 1, v0)
            STEPQ(sa, sb, k0, k1, 3, 1, v1, 2, v0)
            k0 = BC8(ld128(rs, off)); k1 = BC8(ld128(rs, off + 1024)); SB();
            STEPQ(sb, sa, n0, n1, 0, 2, v1, 3, v0)
            v0 = BC8(ld128(rs, off + 2048)); SB();
            TILE(n0, n1, n2, n3, v1, k0, k1, off + 16384)
        }
        p1[2] = cvtpk(e0, e1); p1[3] = cvtpk(e2, e3);
        oo[3] = MFMA32(n3, BC8(p1), oo[3]);
        STAMP(0, 8); CSTAMP1;
        __builtin_amdgcn_s_setprio(3);
        {
            LAS f32x4* ox = (LAS f32x4*)(lds + 16384) + lane;
#pragma unroll
            for (int i = 0; i < 4; ++i) {
                if (i != w) {
#pragma unroll
                    for (int e = 0; e < 4; ++e) ox[((w * 4 + i) * 4 + e) * 64] = f32x4{oo[i][4 * e], oo[i][4 * e + 1], oo[i][4 * e + 2], oo[i][4 * e + 3]};
                } else o = oo[i];
            }
            arrive(cnt + C_OX, lane); waitc(cnt + C_OX, 4u);
#pragma unroll
            for (int i = 1; i < 4; ++i) {
#pragma unroll
                for (int e = 0; e < 4; ++e) { const f32x4 v = ox[((((w + i) & 3) * 4 + w) * 4 + e) * 64]; o[4 * e] += v[0]; o[4 * e + 1] += v[1]; o[4 * e + 2] += v[2]; o[4 * e + 3] += v[3]; }
            }
        }
        float l = hi_half(o[12]);
        float chk = 0.f;
#pragma unroll
        for (int e = 0; e < 16; ++e) chk += o[e] * 0.f;
        if (__builtin_amdgcn_ballot_w64(!(l > 0.25f && l < 1.0e18f && chk == 0.f)) != 0ull) {
            STAMP(0, 14);
            const int g = WS_KV + head * 262144 + lane * 16;
            q1 = set_ref(q1, 0.f, h);
            float m = -INFINITY;
#pragma unroll 1
            for (int t = 0; t < 64; ++t) {
                f32x16 s = MFMA32(BC8(ld128(rs, g + t * 4096)), q0, f32x16{});
                s = MFMA32(BC8(ld128(rs, g + t * 4096 + 1024)), q1, s);
                m = __builtin_fmaxf(m, max16(s));
            }
            q1 = set_ref(q1, xhalf_max(m), h);
            o = f32x16{};
#pragma unroll 1
            for (int t = 0; t < 64; ++t) {
                f32x16 s = MFMA32(BC8(ld128(rs, g + t * 4096)), q0, f32x16{});
                s = MFMA32(BC8(ld128(rs, g + t * 4096 + 1024)), q1, s);
#pragma unroll
                for (int e = 0; e < 4; ++e) { p0[e] = cvtpk(EX(s[2 * e]), EX(s[2 * e + 1])); p1[e] = cvtpk(EX(s[8 + 2 * e]), EX(s[9 + 2 * e])); }
                o = MFMA32(BC8(ld128(rs, g + t * 4096 + 2048)), BC8(p0), o);
                o = MFMA32(BC8(ld128(rs, g + t * 4096 + 3072)), BC8(p1), o);
            }
            l = hi_half(o[12]);
        }
        const float inv = 1.0f / l;
#pragma unroll
        for (int e = 0; e < 4; ++e) { p0[e] = cvtpk(o[2 * e] * inv, o[2 * e + 1] * inv); p1[e] = cvtpk(o[8 + 2 * e] * inv, o[9 + 2 * e] * inv); }
        {
            const int dst = WS_OF + ((((ti * 4 + w) * 16 + head) * 2) * 64 + lane) * 16;
            waitc(cnt + C_R0, 4u);
            st128(rs, dst, p0); st128(rs, dst + 1024, p1);
        }
        publish(rs, cnt + C_O, F_O + (ti * 16 + head) * 128, tag, lane, 1);
        STAMP(0, 9);
        STAMP_FLUSH(0);
    } else {
        __builtin_amdgcn_s_setprio(2);
        const int b = bid, t = tid - 256;
        unsigned wv[8];
        LAS cf* fb = (LAS cf*)(lds + L_F);
        LAS cf* f = fb + w * 7 * RSTR;
        const int l2 = lane < 56 ? lane : 55;
        const int n2 = 2 * t, pos = (n2 >> 6) * RSTR + (n2 & 63);
        const float rr = 0.70710678118654752f;
        const int s2 = l2 >> 3, r0 = l2 & 7, m1 = l2 / 7, s3 = l2 - 7 * m1;
        const int j = 8 * w + (lane >> 3), j0 = (lane >> 3) & 3, d0 = 2 * w + (lane >> 5);
        cf a[8], t448[7], t64[8], t256[8], t32[8], tb1, tb2, tb3, tb4;
        {
            unsigned xv[8];
            const int tc = t < 224 ? t : 223;
#pragma unroll
            for (int i = 0; i < 8; ++i) xv[i] = *(const unsigned*)(bx + (size_t)(256 * i + b) * 448 + 2 * tc);
            {
                const char* wsrc = (const char*)wqkv + 896 * (1 + isv) + 112 * pr;
                const char* xsrc = (const char*)x + (size_t)(ti * 128 + w * 32 + (lane >> 3)) * 896;
                const int pa = 2 * w, pb = w < 3 ? 2 * w + 1 : 6;
                const int ca = pa * 64 + lane, cb_ = pb * 64 + lane;
                const int oa = (ca / 7) * 2688 + (ca % 7) * 16, ob = (cb_ / 7) * 2688 + (cb_ % 7) * 16;
                const int x0 = ((lane & 7) ^ (lane >> 4)) * 16, x1 = ((lane & 7) ^ (4 + (lane >> 4))) * 16;
                auto ldc = [&](int c) __attribute__((always_inline)) {
                    glds16(wsrc + c * (64 * 2688) + oa, lds0 + L_WP + (c * 7 + pa) * 1024);
                    glds16(wsrc + c * (64 * 2688) + ob, lds0 + L_WP + (c * 7 + pb) * 1024);
                    if (c < 5) {
#pragma unroll
                        for (int i = 0; i < 4; ++i) glds16(xsrc + i * (8 * 896) + c * 128 + ((i & 1) ? x1 : x0), lds0 + L_XS + w * 20480 + c * 4096 + i * 1024);
                    }
                };
                ldc(0); ldc(1); ldc(2); ldc(3); ldc(5); ldc(6);
                WAITV(16); arrive(cnt + C_M + 1, lane); ldc(4);
                WAITV(10); arrive(cnt + C_M + 3, lane);
                WAITV(0); arrive(cnt + C_M + 6, lane);
            }
            const int item = bid * 112 + (t < 112 ? t : 111), l = item & 63, fr = item >> 6, hd = fr & 15, cb = fr >> 4;
            const int g = l >> 4, dd = 16 * (g >> 1) + 4 * (g & 1);
            const unsigned short* src = wo + (size_t)(hd * 28) * 448 + cb * 16 + (l & 15);
            if (w < 2) {
#pragma unroll
                for (int e = 0; e < 8; ++e) { const int d = dd + 8 * (e >> 2) + (e & 3); wv[e] = src[(d < 28 ? d : 0) * 448]; }
            }
#pragma unroll
            for (int i = 1; i < 8; ++i) { if (i < 7) t448[i] = tw(lane * i, 1.f / 448); t64[i] = tw(r0 * i, 1.f / 64); t256[i] = tw(j * i, 1.f / 256); t32[i] = tw(j0 * i, 1.f / 32); }
            tb1 = tw(b, 1.f / 2048); tb2 = tw(2 * b, 1.f / 2048); tb3 = tw(3 * b, 1.f / 2048); tb4 = tw(b, 1.f / 512);
#pragma unroll
            for (int i = 1; i < 8; ++i) { if (i < 7) asm volatile("" : "+v"(t448[i])); asm volatile("" : "+v"(t64[i]), "+v"(t256[i]), "+v"(t32[i])); }
            asm volatile("" : "+v"(tb1), "+v"(tb2), "+v"(tb3), "+v"(tb4));
            if (t < 224) {
                cf v[8];
#pragma unroll
                for (int i = 0; i < 8; ++i) v[i] = cf{bflo(xv[i]), bfhi(xv[i])};
                cf p0 = v[0] + v[4], m0 = v[0] - v[4], p1 = v[1] + v[5], n1 = v[1] - v[5];
                cf p2 = v[2] + v[6], m2 = v[2] - v[6], p3 = v[3] + v[7], m3 = v[3] - v[7];
                cf e0 = p0 + p2, e1 = p1 + p3;
                cf z0 = e0 + e1, z4 = e0 - e1;
                cf z2r = p0 - p2, z2i = p3 - p1;
                cf qa = (n1 - m3) * rr, qb = (n1 + m3) * rr;
                cf z1r = m0 + qa, z1i = -m2 - qb, z3r = m0 - qa, z3i = m2 - qb;
                const cf w1 = tb1, w2 = tb2, w3 = tb3;
                cf a0, a1;
                *(LAS f32x4*)(fb + pos) = f32x4{z0.x, z4.x, z0.y, z4.y};
                a0 = cmul<-1>(cf{z1r.x, z1i.x}, w1); a1 = cmul<-1>(cf{z1r.y, z1i.y}, w1);
                *(LAS f32x4*)(fb + 7 * RSTR + pos) = f32x4{a0.x, a0.y, a1.x, a1.y};
                a0 = cmul<-1>(cf{z2r.x, z2i.x}, w2); a1 = cmul<-1>(cf{z2r.y, z2i.y}, w2);
                *(LAS f32x4*)(fb + 14 * RSTR + pos) = f32x4{a0.x, a0.y, a1.x, a1.y};
                a0 = cmul<-1>(cf{z3r.x, z3i.x}, w3); a1 = cmul<-1>(cf{z3r.y, z3i.y}, w3);
                *(LAS f32x4*)(fb + 21 * RSTR + pos) = f32x4{a0.x, a0.y, a1.x, a1.y};
            }
            arrive(cnt + C_B0, lane);
            if (w >= 2 && lane < 40) {
                int e = (8 * (8 * (bid >> 3) + lane / 5) + xcd) * 225 + 7 * xcd + (lane % 5) * 64;
                e = e < 2048 * 225 ? e : 2048 * 225 - 1;
                wv[0] = (w == 2 ? fwr : fwi)[e];
            }
            waitc(cnt + C_B0, 4u);
        }
        STAMP(0, 1);
#pragma unroll
        for (int p = 0; p < 7; ++p) a[p] = f[p * RSTR + lane];
        dft7<-1>(a);
#pragma unroll
        for (int s = 1; s < 7; ++s) a[s] = cmul<-1>(a[s], t448[s]);
#pragma unroll
        for (int s = 0; s < 7; ++s) f[s * RSTR + lane] = a[s];
        WAITL();
#pragma unroll
        for (int i = 0; i < 8; ++i) a[i] = f[s2 * RSTR + 8 * i + r0];
        dft8<-1>(a);

#pragma unroll
        for (int i = 1; i < 8; ++i) a[i] = cmul<-1>(a[i], t64[i]);
        if (lane < 56) {
#pragma unroll
            for (int i = 0; i < 8; ++i) f[s2 * RSTR + 8 * i + r0] = a[i];
        }
        WAITL();
#pragma unroll
        for (int i = 0; i < 8; ++i) a[i] = f[s3 * RSTR + 8 * m1 + i];
        dft8<-1>(a);
        {
            LAS cf* y = (LAS cf*)(lds + L_YA);
            if (w != 0) {
                if (lane < 56) {
#pragma unroll
                    for (int i = 0; i < 4; ++i) y[w * 225 + lane + 56 * i] = a[i];
#pragma unroll
                    for (int i = 4; i < 8; ++i) y[(8 - w) * 225 + 448 - lane - 56 * i] = cconj(a[i]);
                    if (lane == 0) { y[w * 225 + 224] = a[4]; y[(8 - w) * 225] = cconj(a[0]); }
                }
            } else {
                WAITL();
                if (lane < 56) {
#pragma unroll
                    for (int i = 0; i < 8; ++i) f[lane + 56 * i] = a[i];
                }
                if (lane == 0) f[448] = a[0];
                WAITL();
                const cf w4 = cf{tb4.y, -tb4.x} * 0.5f;
#pragma unroll
                for (int i = 0; i < 4; ++i) {
                    const int k = lane + 64 * i;
                    if (k <= 224) {
                        const cf u = f[k], v = cconj(f[448 - k]);
                        y[k] = (u + v) * 0.5f;
                        y[4 * 225 + k] = cmul<-1>(u - v, w4);
                    }
                }
            }
            arrive(cnt + C_B1, lane); waitc(cnt + C_B1, 4u);
#pragma unroll
            for (int i = 0; i < 4; ++i) { const int e = t + 256 * i; if (e < 900) st128(rs, WS_Y1 + b * (NQ * 8) + e * 16, ((LAS u32x4*)y)[e]); }
            const bool pad = ((bid * 112 + t) & 48) == 48;
            if (t < 112) st128(rs, WS_WO + (bid * 112 + t) * 16, u32x4{wv[0] | (wv[1] << 16), wv[2] | (wv[3] << 16), pad ? 0u : wv[4] | (wv[5] << 16), pad ? 0u : wv[6] | (wv[7] << 16)});
        }
        asm volatile("s_waitcnt vmcnt(0)\n\tv_readfirstlane_b32 %0, %1" : "=s"(tag) : "v"(genv) : "memory");
        asm volatile("" :: "v"(wv[0]));
        tag += 1u;
        STAMP(0, 2);
        publish(rs, cnt + C_B3, F_Y1 + bid * 4, tag, lane, 8);
        STAMP(0, 3);
        waitc(cnt + C_PK, 1u);
        {
            const int grp = (bid & 7) * 29 + (bid >> 3);
            const bool act = (bid >> 3) < 29 && grp < 225;
            const int col = lane & 7, q = (act ? grp : 0) * 8 + col, c = q / 225, k2 = q - c * 225, cs = c > 4 ? c - 8 : c;
            {
                const int dst = WS_OF + ((((ti * 4 + w) * 16 + head) * 2) * 64 + lane) * 16;
                st128(rs, dst, u32x4{}); st128(rs, dst + 1024, u32x4{});
            }
            unsigned fr_[8], fi_[8];
#pragma unroll
            for (int i = 0; i < 8; ++i) {
                const int d = d0 + 8 * (j0 + 4 * (i >> 2)) + 64 * (i & 3), k1 = (cs + 8 * d) & 2047;
                fr_[i] = fwr[k1 * 225 + k2]; fi_[i] = fwi[k1 * 225 + k2];
                __builtin_amdgcn_s_sleep(3); FENCE();
            }
            if (w == 0) {
                poll128(rs, F_Y1 + xcd * FREP + lane * 16, tag);
                arrive(cnt + C_B2, lane);
            } else waitc(cnt + C_B2, 1u);
            if (t < 112) { wv[0] = ld32(rs, WS_WO + ((bid >> 3) * 112 + t) * 128); asm volatile("" :: "v"(wv[0])); }
            STAMP(0, 4);
            LAS cf* mb = fb;
#pragma unroll
            for (int i = 0; i < 8; ++i) a[i] = ld64(rs, WS_Y1 + ((32 * i + j) * NQ + q) * 8);
            if (act) {
                dft8<-1>(a);
                STAMP(0, 5);
#pragma unroll
                for (int i = 1; i < 8; ++i) a[i] = cmul<-1>(a[i], t256[i]);
#pragma unroll
                for (int i = 0; i < 8; ++i) mb[(i * 32 + j) * 8 + col] = a[i];
                arrive(cnt + C_B4, lane); waitc(cnt + C_B4, 4u);
#pragma unroll
                for (int i = 0; i < 8; ++i) a[i] = mb[(d0 * 32 + 4 * i + j0) * 8 + col];
                dft8<-1>(a);
#pragma unroll
                for (int i = 1; i < 8; ++i) a[i] = cmul<-1>(a[i], t32[i]);
#pragma unroll
                for (int i = 0; i < 8; ++i) mb[(d0 * 32 + 4 * i + j0) * 8 + col] = a[i];
                WAITL();
#pragma unroll
                for (int i = 0; i < 8; ++i) a[i] = mb[(d0 * 32 + 4 * (j0 + 4 * (i >> 2)) + (i & 3)) * 8 + col];
                dft4<-1>(a[0], a[1], a[2], a[3]); dft4<-1>(a[4], a[5], a[6], a[7]);
#pragma unroll
                for (int i = 0; i < 8; ++i) a[i] = cmul<1>(a[i], cf{bflo(fi_[i]), bflo(fr_[i])});
                dft4<1>(a[0], a[1], a[2], a[3]); dft4<1>(a[4], a[5], a[6], a[7]);
#pragma unroll
                for (int i = 0; i < 8; ++i) mb[(d0 * 32 + 4 * (j0 + 4 * (i >> 2)) + (i & 3)) * 8 + col] = a[i];
                WAITL();
#pragma unroll
                for (int i = 0; i < 8; ++i) a[i] = mb[(d0 * 32 + 4 * i + j0) * 8 + col];
#pragma unroll
                for (int i = 1; i < 8; ++i) a[i] = cmul<1>(a[i], t32[i]);
                dft8<1>(a);
#pragma unroll
                for (int i = 0; i < 8; ++i) mb[(d0 * 32 + 4 * i + j0) * 8 + col] = a[i];
                arrive(cnt + C_B5, lane); waitc(cnt + C_B5, 4u);
#pragma unroll
                for (int i = 0; i < 8; ++i) a[i] = mb[(i * 32 + j) * 8 + col];
#pragma unroll
                for (int i = 1; i < 8; ++i) a[i] = cmul<1>(a[i], t256[i]);
                dft8<1>(a);
#pragma unroll
                for (int i = 0; i < 8; ++i) st64(rs, WS_Y2 + ((32 * i + j) * NQ + q) * 8, a[i]);
            }
        }
        STAMP(0, 6);
        publish(rs, cnt + C_R0, F_Y2 + bid * 4, tag, lane, 8);
        STAMP(0, 7);
        bf16x8 wa[16], wb[16];
        const int cb0 = cq * 7 + w, cb1 = cq * 7 + (w < 3 ? w + 4 : 6);
#pragma unroll
        for (int i = 0; i < 16; ++i) { wa[i] = BC8(ld128(rs, WS_WO + ((cb0 * 16 + i) * 64 + lane) * 16)); wb[i] = BC8(ld128(rs, WS_WO + ((cb1 * 16 + i) * 64 + lane) * 16)); __builtin_amdgcn_s_sleep(5); FENCE(); }
        waitc(cnt + C_R0, 4u);
        STAMP(0, 8);
        if (w == 0) {
            poll128(rs, F_Y2 + xcd * FREP + lane * 16, tag);
            arrive(cnt + C_PY, lane);
            if (bid == 0 && lane == 0) st32(rs, F_GEN, tag);
        } else waitc(cnt + C_PY, 1u);
        {
            const int y = WS_Y2 + b * (NQ * 8);
            cf z[8];
            const int ya = y + (w * 225) * 8, yb = y + ((w ? 8 - w : 0) * 225) * 8;
#pragma unroll
            for (int i = 0; i < 8; ++i) a[i] = ld64(rs, i < 4 ? ya + (l2 + 56 * i) * 8 : yb + (448 - l2 - 56 * i) * 8);
            if (w) { z[0] = ld64(rs, yb); z[4] = ld64(rs, ya + 224 * 8); }
            else {
#pragma unroll
                for (int i = 0; i < 8; ++i) z[i] = ld64(rs, y + (900 + (i < 4 ? l2 + 56 * i : 448 - l2 - 56 * i)) * 8);
            }
            waitc(cnt + C_OX, 1u);
            STAMP(0, 9);
#pragma unroll
            for (int i = 4; i < 8; ++i) a[i] = cconj(a[i]);
            if (w) { if (lane == 0) { a[0] = (a[0] + cconj(z[0])) * 0.5f; a[4] = (a[4] + z[4]) * 0.5f; } }
            else {
                const cf w4 = tb4;
#pragma unroll
                for (int i = 0; i < 8; ++i) {
                    cf z4 = cmul<1>(z[i], w4);
                    if (i >= 4) z4 = cconj(z4);
                    if ((i & 3) == 0 && lane == 0) { a[i].y = 0.f; z4.y = 0.f; }
                    a[i] = addrot<1>(a[i], z4);
                }
            }
        }
        dft8<1>(a);
        if (lane < 56) {
#pragma unroll
            for (int i = 0; i < 8; ++i) f[s3 * RSTR + 8 * m1 + i] = a[i];
        }
        WAITL();
#pragma unroll
        for (int i = 0; i < 8; ++i) a[i] = f[s2 * RSTR + 8 * i + r0];
#pragma unroll
        for (int i = 1; i < 8; ++i) a[i] = cmul<1>(a[i], t64[i]);
        dft8<1>(a);
        if (lane < 56) {
#pragma unroll
            for (int i = 0; i < 8; ++i) f[s2 * RSTR + 8 * i + r0] = a[i];
        }
        WAITL();
#pragma unroll
        for (int s = 0; s < 7; ++s) a[s] = f[s * RSTR + lane];
#pragma unroll
        for (int s = 1; s < 7; ++s) a[s] = cmul<1>(a[s], t448[s]);
        dft7<1>(a);
#pragma unroll
        for (int p = 0; p < 7; ++p) f[p * RSTR + lane] = a[p];
        arrive(cnt + C_B6, lane); waitc(cnt + C_B6, 4u);
        if (t < 224) {
            const float sc = 1.0f / (2048.0f * 448.0f), sc2 = 2.f * sc;
            const f32x4 u0 = *(LAS f32x4*)(fb + pos), u1 = *(LAS f32x4*)(fb + 7 * RSTR + pos), u2 = *(LAS f32x4*)(fb + 14 * RSTR + pos), u3 = *(LAS f32x4*)(fb + 21 * RSTR + pos);
            const cf w1 = tb1 * sc2, w2 = tb2 * sc2, w3 = tb3 * sc2;
            const cf z0 = cf{u0[0], u0[2]} * sc, z4 = cf{u0[1], u0[3]} * sc;
            cf a0 = cmul<1>(cf{u1[0], u1[1]}, w1), a1 = cmul<1>(cf{u1[2], u1[3]}, w1);
            const cf z1r = cf{a0.x, a1.x}, z1i = cf{a0.y, a1.y};
            a0 = cmul<1>(cf{u2[0], u2[1]}, w2); a1 = cmul<1>(cf{u2[2], u2[3]}, w2);
            const cf z2r = cf{a0.x, a1.x}, z2i = cf{a0.y, a1.y};
            a0 = cmul<1>(cf{u3[0], u3[1]}, w3); a1 = cmul<1>(cf{u3[2], u3[3]}, w3);
            const cf z3r = cf{a0.x, a1.x}, z3i = cf{a0.y, a1.y};
            const cf e0 = z0 + z4, e1 = z0 - z4;
            const cf p0 = e0 + z2r, p2 = e0 - z2r, p1 = e1 - z2i, p3 = e1 + z2i;
            const cf m0 = z1r + z3r, m2 = z3i - z1i;
            const cf qa = (z1r - z3r) * rr, qb = (z1i + z3i) * rr;
            const cf mm1 = qa - qb, m3 = -qa - qb;
            const cf yv[8] = {p0 + m0, p1 + mm1, p2 + m2, p3 + m3, p0 - m0, p1 - mm1, p2 - m2, p3 - m3};
#pragma unroll
            for (int i = 0; i < 8; ++i) *(unsigned*)(out + (size_t)(256 * i + b) * 896 + 448 + n2) = cvtpk(yv[i].x, yv[i].y);
        }
        STAMP(0, 10);
        waitc(cnt + C_OX, 4u);
        __builtin_amdgcn_s_setprio(3);
        if (w == 0) { poll32(rs, F_O + ((otile >> 2) * 16 + (lane & 15)) * 128, tag); arrive(cnt + C_PO, lane); }
        else waitc(cnt + C_PO, 1u);
        STAMP(0, 11);
        u32x4 ov[8];
#pragma unroll
        for (int i = 0; i < 8; ++i) ov[i] = ld128(rs, WS_OF + (otile * 2048 + (i * 4 + w) * 64 + lane) * 16);
#pragma unroll
        for (int i = 0; i < 8; ++i) ((LAS u32x4*)(lds + 98304))[(i * 4 + w) * 64 + lane] = ov[i];
        arrive(cnt + C_OS, lane); waitc(cnt + C_OS, 4u);
        STAMP(0, 12);
        {
            const int g = lane >> 4;
            const LAS bf16x8* op = (const LAS bf16x8*)(lds + 98304) + (g >> 1) * 64 + (g & 1) * 32 + (lane & 15);
            f32x4 c0 = f32x4{}, c1 = f32x4{}, c2 = f32x4{}, c3 = f32x4{};
#pragma unroll
            for (int i = 0; i < 16; ++i) {
                const bf16x8 a0 = op[i * 128], a1 = op[i * 128 + 16];
                c0 = MFMA16(wa[i], a0, c0); c1 = MFMA16(wa[i], a1, c1); c2 = MFMA16(wb[i], a0, c2); c3 = MFMA16(wb[i], a1, c3);
            }
            unsigned short* dst = out + (size_t)(otile * 32 + (lane & 15)) * 896 + g * 4;
            *(u32x2*)(dst + cb0 * 16) = u32x2{cvtpk(c0[0], c0[1]), cvtpk(c0[2], c0[3])};
            *(u32x2*)(dst + cb0 * 16 + 16 * 896) = u32x2{cvtpk(c1[0], c1[1]), cvtpk(c1[2], c1[3])};
            if (w < 3) {
                *(u32x2*)(dst + cb1 * 16) = u32x2{cvtpk(c2[0], c2[1]), cvtpk(c2[2], c2[3])};
                *(u32x2*)(dst + cb1 * 16 + 16 * 896) = u32x2{cvtpk(c3[0], c3[1]), cvtpk(c3[2], c3[3])};
            }
        }
        STAMP(0, 13); STAMP_FLUSH(0);
    }
}

extern "C" void kernel_launch(void* const* d_in, const int* in_sizes, int n_in, void* d_out, int out_size, void* d_ws,
                              size_t ws_size, hipStream_t stream) {
    (void)hipFuncSetAttribute((const void*)fused_kernel, hipFuncAttributeMaxDynamicSharedMemorySize, L_END);
    fused_kernel<<<256, 512, L_END, stream>>>((const unsigned short*)d_in[0], (const unsigned short*)d_in[1], (const unsigned short*)d_in[2],
                                              (const unsigned short*)d_in[3], (const unsigned short*)d_in[4], (const unsigned short*)d_in[5],
                                              (unsigned short*)d_out, (char*)d_ws);
}
```

```cpp
#include <hip/hip_runtime.h>
#include <hip/hip_bf16.h>
#include <stdint.h>

typedef __attribute__((ext_vector_type(8))) short bf16x8;
typedef __attribute__((ext_vector_type(4))) short s16x4;
typedef __attribute__((ext_vector_type(16))) float f32x16;
typedef __attribute__((ext_vector_type(4))) float f32x4;
typedef __attribute__((ext_vector_type(4))) unsigned u32x4;
typedef __attribute__((ext_vector_type(2))) unsigned u32x2;
typedef __attribute__((ext_vector_type(2))) float cf;
#define LAS __attribute__((address_space(3)))
#if defined(__HIP_DEVICE_COMPILE__)
#define NOPK __attribute__((target("no-packed-fp32-ops")))
#else
#define NOPK
#endif
#define DEVI __device__ __forceinline__
#define MFMA32(a, b, c) __builtin_amdgcn_mfma_f32_32x32x16_bf16(a, b, c, 0, 0, 0)
#define MFMA16(a, b, c) __builtin_amdgcn_mfma_f32_16x16x32_bf16(a, b, c, 0, 0, 0)

constexpr int NQ = 1800;
constexpr int F_GEN = 0, F_KV = 256, F_O = F_KV + 256 * 128, F_Y1 = F_O + 256 * 128, FREP = 1024 + 256, F_Y2 = F_Y1 + 8 * FREP;
constexpr int WS_KV = 131072;
constexpr int WS_WO = WS_KV + 16 * 64 * 4 * 1024;
constexpr int WS_OF = WS_WO + 28 * 16 * 1024;
constexpr int WS_Y1 = WS_OF + 64 * 16 * 2 * 1024;
constexpr int WS_Y2 = WS_Y1;
constexpr int WS_END = WS_Y1 + 256 * NQ * 8;
constexpr float QSCALE = 0.18898223650461362f * 1.4426950408889634f;

typedef __attribute__((ext_vector_type(2))) __bf16 bf16x2_t;
DEVI unsigned cvtpk(float lo, float hi) { return __builtin_bit_cast(unsigned, __builtin_convertvector(cf{lo, hi}, bf16x2_t)); }
DEVI float bflo(unsigned u) { return __uint_as_float(u << 16); }
DEVI float bfhi(unsigned u) { return __uint_as_float(u & 0xffff0000u); }
DEVI void glds16(const void* g, unsigned lds_base) {
    unsigned sv;
    asm volatile("s_mov_b32 %0, m0\n\ts_mov_b32 m0, %2\n\ts_nop 0\n\tglobal_load_lds_dwordx4 %1, off\n\ts_mov_b32 m0, %0"
                 : "=&s"(sv) : "v"(g), "s"(__builtin_amdgcn_readfirstlane(lds_base)) : "memory");
}
#define WAITV(N) asm volatile("s_waitcnt vmcnt(" #N ")" ::: "memory")
#define WAITL() asm volatile("s_waitcnt lgkmcnt(0)" ::: "memory")
#define BAR() asm volatile("s_waitcnt lgkmcnt(0)\n\ts_barrier" ::: "memory")

template <int DIR> DEVI cf cmul(cf a, cf w) {
    return DIR > 0 ? cf{__builtin_fmaf(a.x, w.y, -a.y * w.x), __builtin_fmaf(a.y, w.y, a.x * w.x)}
                   : cf{__builtin_fmaf(a.x, w.y, a.y * w.x), __builtin_fmaf(a.y, w.y, -a.x * w.x)};
}
template <int DIR> DEVI cf addrot(cf t, cf x) { return DIR > 0 ? cf{t.x - x.y, t.y + x.x} : cf{t.x + x.y, t.y - x.x}; }
DEVI cf cconj(cf a) { return cf{a.x, -a.y}; }
DEVI cf tw(int num, float inv_den) {
    const float x = (float)num * inv_den;
    return cf{__builtin_amdgcn_sinf(x), __builtin_amdgcn_cosf(x)};
}
template <int DIR> DEVI void dft4(cf& a0, cf& a1, cf& a2, cf& a3) {
    cf t0 = a0 + a2, t1 = a0 - a2, t2 = a1 + a3, t3 = a1 - a3;
    a0 = t0 + t2; a2 = t0 - t2; a1 = addrot<DIR>(t1, t3); a3 = addrot<-DIR>(t1, t3);
}
template <int DIR> DEVI void dft8(cf* a) {
    cf e0 = a[0], e1 = a[2], e2 = a[4], e3 = a[6], o0 = a[1], o1 = a[3], o2 = a[5], o3 = a[7];
    dft4<DIR>(e0, e1, e2, e3); dft4<DIR>(o0, o1, o2, o3);
    const float r = 0.70710678118654752f;
    o1 = addrot<DIR>(o1, o1); o3 = addrot<-DIR>(o3, o3);
    a[0] = e0 + o0; a[4] = e0 - o0; a[1] = e1 + o1 * r; a[5] = e1 - o1 * r;
    a[2] = addrot<DIR>(e2, o2); a[6] = addrot<-DIR>(e2, o2); a[3] = e3 - o3 * r; a[7] = e3 + o3 * r;
}
template <int DIR> DEVI void dft7(cf* a) {
    const float c1 = 0.62348980185873353f, c2 = -0.22252093395631440f, c3 = -0.90096886790241913f;
    const float s1 = 0.78183148246802981f, s2 = 0.97492791218182361f, s3 = 0.43388373911755812f;
    cf t1 = a[1] + a[6], t2 = a[2] + a[5], t3 = a[3] + a[4], u1 = a[1] - a[6], u2 = a[2] - a[5], u3 = a[3] - a[4];
    cf A1 = a[0] + t1 * c1 + t2 * c2 + t3 * c3, A2 = a[0] + t1 * c2 + t2 * c3 + t3 * c1, A3 = a[0] + t1 * c3 + t2 * c1 + t3 * c2;
    cf B1 = u1 * s1 + u2 * s2 + u3 * s3, B2 = u1 * s2 - u2 * s3 - u3 * s1, B3 = u1 * s3 - u2 * s1 + u3 * s2;
    a[0] = a[0] + t1 + t2 + t3;
    a[1] = addrot<DIR>(A1, B1); a[6] = addrot<-DIR>(A1, B1); a[2] = addrot<DIR>(A2, B2); a[5] = addrot<-DIR>(A2, B2);
    a[3] = addrot<DIR>(A3, B3); a[4] = addrot<-DIR>(A3, B3);
}
constexpr int RSTR = 72;
constexpr int RFB = 7 * RSTR * 8;

typedef __amdgpu_buffer_rsrc_t rsrc_t;
DEVI void st128(rsrc_t r, int off, u32x4 v) { __builtin_amdgcn_raw_buffer_store_b128(v, r, off, 0, 16); }
DEVI void st64(rsrc_t r, int off, cf v) { __builtin_amdgcn_raw_buffer_store_b64(__builtin_bit_cast(u32x2, v), r, off, 0, 16); }
DEVI void st32(rsrc_t r, int off, unsigned v) { __builtin_amdgcn_raw_buffer_store_b32(v, r, off, 0, 16); }
DEVI u32x4 ld128(rsrc_t r, int off) { return __builtin_amdgcn_raw_buffer_load_b128(r, off, 0, 16); }
DEVI cf ld64(rsrc_t r, int off) { return __builtin_bit_cast(cf, __builtin_amdgcn_raw_buffer_load_b64(r, off, 0, 16)); }
DEVI unsigned ld32(rsrc_t r, int off) { return __builtin_amdgcn_raw_buffer_load_b32(r, off, 0, 16); }
#define FENCE() asm volatile("" ::: "memory")
constexpr int SPIN_CAP = 1 << 18;
DEVI void arrive(LAS unsigned* c, int) {
    asm volatile("s_waitcnt lgkmcnt(0)\n\ts_mov_b64 exec, 1\n\tds_add_u32 %0, %1\n\ts_mov_b64 exec, -1" :: "v"(c), "v"(1u) : "memory");
}
DEVI unsigned arrive_rtn(LAS unsigned* c) {
    unsigned v, r;
    asm volatile("s_waitcnt lgkmcnt(0)\n\ts_mov_b64 exec, 1\n\tds_add_rtn_u32 %0, %2, %3\n\ts_waitcnt lgkmcnt(0)\n\tv_readfirstlane_b32 %1, %0\n\ts_mov_b64 exec, -1"
                 : "=&v"(v), "=s"(r) : "v"(c), "v"(1u) : "memory");
    return r;
}
DEVI void waitc(LAS unsigned* c, unsigned n) {
    unsigned v, sv, it;
    asm volatile("s_mov_b32 %2, 0x200000\n"
                 "1:\n\tds_read_b32 %0, %3\n\ts_waitcnt lgkmcnt(0)\n\tv_readfirstlane_b32 %1, %0\n\ts_cmp_ge_u32 %1, %4\n\ts_cbranch_scc1 2f\n\t"
                 "s_sleep 3\n\ts_sub_u32 %2, %2, 1\n\ts_cbranch_scc0 1b\n2:"
                 : "=&v"(v), "=&s"(sv), "=&s"(it) : "v"(c), "s"(n) : "memory", "scc");
}
DEVI void publish(rsrc_t r, LAS unsigned* c, int flag_off, unsigned tag, int lane, int nrep) {
    WAITV(0);
    if (arrive_rtn(c) == 3u && lane < nrep) st32(r, flag_off + lane * FREP, tag);
}
DEVI void poll32(rsrc_t r, int off, unsigned tag) {
#pragma unroll 1
    for (int i = 0; i < SPIN_CAP; ++i) { if (__builtin_amdgcn_ballot_w64(ld32(r, off) != tag) == 0ull) break; __builtin_amdgcn_s_sleep(2); FENCE(); }
    FENCE();
}
DEVI void poll128(rsrc_t r, int off, unsigned tag) {
#pragma unroll 1
    for (int i = 0; i < SPIN_CAP; ++i) {
        const u32x4 v = ld128(r, off);
        if (__builtin_amdgcn_ballot_w64(!(v[0] == tag && v[1] == tag && v[2] == tag && v[3] == tag)) == 0ull) break;
        __builtin_amdgcn_s_sleep(10); FENCE();
    }
    FENCE();
}

DEVI float xhalf_max(float m) {
    auto rr = __builtin_amdgcn_permlane32_swap(__float_as_uint(m), __float_as_uint(m), false, false);
    return __builtin_fmaxf(__uint_as_float(rr[0]), __uint_as_float(rr[1]));
}
DEVI float hi_half(float m) {
    auto rr = __builtin_amdgcn_permlane32_swap(__float_as_uint(m), __float_as_uint(m), false, false);
    return __uint_as_float(rr[1]);
}
DEVI float max16(const f32x16& s) {
    float m = __builtin_fmaxf(s[0], s[1]);
#pragma unroll
    for (int i = 2; i < 16; ++i) m = __builtin_fmaxf(m, s[i]);
    return m;
}
DEVI bf16x8 set_ref(bf16x8 q1, float m, int h) {
    const float nm = -m;
    const unsigned b0 = __float_as_uint(nm) & 0xffff0000u; const float r1 = nm - __uint_as_float(b0);
    const unsigned b1 = __float_as_uint(r1) & 0xffff0000u; const float r2 = r1 - __uint_as_float(b1);
    const unsigned b2 = __float_as_uint(r2) & 0xffff0000u;
    u32x4 u = __builtin_bit_cast(u32x4, q1);
    if (h) { u[2] = (b0 >> 16) | b1; u[3] = b2 >> 16; }
    return __builtin_bit_cast(bf16x8, u);
}
DEVI bf16x8 trfrag(const LAS char* p, int rowstride) {
    s16x4 lo = __builtin_amdgcn_ds_read_tr16_b64_v4i16((LAS s16x4*)p);
    s16x4 hi = __builtin_amdgcn_ds_read_tr16_b64_v4i16((LAS s16x4*)(p + 4 * rowstride));
    return __builtin_shufflevector(lo, hi, 0, 1, 2, 3, 4, 5, 6, 7);
}
#define SB() __builtin_amdgcn_sched_barrier(0)
#define EX(x) __builtin_amdgcn_exp2f(x)
#define BC8(x) __builtin_bit_cast(bf16x8, x)

#define HX 2
constexpr int L_WP = 0, L_XS = 50240, L_F = L_XS + 4 * 20480, L_YA = L_F + 16384, L_C = L_YA + 14400, L_END = L_C + 256;
enum { C_Q = 0, C_PK, C_PO, C_PY, C_WQ, C_R0, C_O, C_OS, C_QS, C_OX, C_B0, C_B1, C_B2, C_B3, C_B4, C_B5, C_B6, C_M = 40 };

__global__ void NOPK __launch_bounds__(512, 2) fused_kernel(const unsigned short* __restrict__ x, const unsigned short* __restrict__ wqkv,
                                                       const unsigned short* __restrict__ wo, const unsigned short* __restrict__ bx,
                                                       const unsigned short* __restrict__ fwr, const unsigned short* __restrict__ fwi,
                                                       unsigned short* __restrict__ out, char* ws) {
    extern __shared__ __attribute__((aligned(16))) char lds[];
    const int tid = threadIdx.x, lane = tid & 63, wid = __builtin_amdgcn_readfirstlane(tid >> 6);
    const int bid = blockIdx.x, xcd = bid & 7;
    const int hl = (bid >> 3) & (HX - 1), head = (xcd / (HX / 2)) * HX + hl, ti = (xcd & (HX / 2 - 1)) * (32 / HX) + (bid >> 3) / HX;
    const int isv = hl / (HX / 2), pr = (head - hl) / 2 + (hl & (HX / 2 - 1));
    const unsigned lds0 = (unsigned)(uintptr_t)lds;
    const rsrc_t rs = __builtin_amdgcn_make_buffer_rsrc((void*)ws, (short)0, WS_END, 0x00020000);
    LAS unsigned* cnt = (LAS unsigned*)(lds + L_C);
    unsigned genv;
    asm volatile("global_load_dword %0, %1, %2 sc1" : "=v"(genv) : "v"(0), "s"(ws) : "memory");
    if (tid < 64) cnt[tid] = 0u;
    BAR();
    const int otile = xcd * 8 + (bid >> 5), cq = (bid >> 3) & 3;
    const int w = wid & 3;
    const int r = lane & 31, h = lane >> 5;
    unsigned tag;
    if (wid < 4) {
        __builtin_amdgcn_s_setprio(3);
        if (w == 0) {
            const unsigned p0_ = ld32(rs, (lane < 41 ? lane : 40) * 262144 + 128), p1_ = out[(lane & 15) * 114688];
            asm volatile("" :: "v"(p0_), "v"(p1_));
        }
        const char* xsrc = (const char*)x + (size_t)(ti * 128 + w * 32 + (lane >> 3)) * 896;
        u32x4 xt[8];
#pragma unroll
        for (int i = 0; i < 8; ++i) {
            const char* g = xsrc + (i & 3) * (8 * 896) + (5 + (i >> 2)) * 128 + (((lane & 7) ^ ((4 * (i & 3) + (lane >> 4)) & 7)) * 16);
            xt[i] = *(const u32x4*)g;
        }
        const LAS char* xr = (const LAS char*)lds + L_XS + w * 20480 + r * 128;
        const int xk = (r >> 1) & 7;
        const int trow = 8 * h + ((lane & 15) >> 2), tcol = ((lane >> 4) & 1) * 32 + (lane & 3) * 8;
        const LAS char* wr = (const LAS char*)lds + L_WP + trow * 112 + tcol;
        f32x16 acc0 = f32x16{}, acc1 = f32x16{};
        bf16x8 xf[28], wf[8];
        u32x4 wq[7];
        const char* qs = (const char*)wqkv + ((56 * head) & ~15) + (size_t)(w * 112 + (lane >> 2)) * 2688 + (lane & 3) * 16;
        asm volatile("s_waitcnt vmcnt(8)\n\tv_readfirstlane_b32 %0, %1" : "=s"(tag) : "v"(genv) : "memory"); waitc(cnt + C_M + 1, 4u);
        tag += 1u;
#pragma unroll
        for (int kk = 0; kk < 4; ++kk) { wf[2 * kk] = trfrag(wr + kk * 16 * 112, 112); wf[2 * kk + 1] = trfrag(wr + kk * 16 * 112 + 56, 112); }
#pragma unroll
        for (int c = 0; c < 7; ++c) {
#define XRD(C) _Pragma("unroll") for (int kk = 0; kk < 4; ++kk) xf[4 * (C) + kk] = *(const LAS bf16x8*)(xr + ((C) % 5) * 4096 + (((2 * kk + h) ^ xk) * 16));
            if (c == 0) XRD(0)
            if (c == 1) waitc(cnt + C_M + 3, 4u); else if (c == 3) waitc(cnt + C_M + 6, 4u);
            if (c == 3) {
#pragma unroll
                for (int i = 0; i < 8; ++i) *(LAS u32x4*)(lds + L_XS + w * 20480 + i * 1024 + lane * 16) = xt[i];
                FENCE();
            }
            if (c < 6) XRD(c + 1)
            if (c >= 1 && c < 5) { wq[2 * c - 2] = *(const u32x4*)(qs + (2 * c - 2) * (16 * 2688)); if (c < 4) wq[2 * c - 1] = *(const u32x4*)(qs + (2 * c - 1) * (16 * 2688)); }
#define WNEXT if (c < 6) { wf[2 * kk] = trfrag(wr + ((c + 1) * 64 + kk * 16) * 112, 112); wf[2 * kk + 1] = trfrag(wr + ((c + 1) * 64 + kk * 16) * 112 + 56, 112); } SB();
            SB();
            {
#pragma unroll
                for (int kk = 0; kk < 4; ++kk) { acc0 = MFMA32(wf[2 * kk], xf[4 * c + kk], acc0); acc1 = MFMA32(wf[2 * kk + 1], xf[4 * c + kk], acc1); SB(); WNEXT }
            }
        }
        {
            const int h0 = 2 * pr, dst = WS_KV + (((h0 * 64 + ti * 4 + w) * 4 + (isv ? 2 : 0)) * 64 + lane) * 16;
#pragma unroll
            for (int j = 0; j < 2; ++j) {
                const f32x16 a = j ? acc1 : acc0;
                u32x4 f0, f1;
#pragma unroll
                for (int e = 0; e < 4; ++e) { f0[e] = cvtpk(a[2 * e], a[2 * e + 1]); f1[e] = cvtpk(a[8 + 2 * e], a[9 + 2 * e]); }
                if (h) { f1[2] = isv ? 0x00003f80u : 0x3f803f80u; f1[3] = isv ? 0u : 0x00003f80u; }
                if (isv) {
                    LAS char* tb = (LAS char*)lds + L_XS + w * 20480 + 8192 + j * 2048;
                    LAS u32x2* tw_ = (LAS u32x2*)(tb + r * 64 + h * 8);
                    tw_[0] = u32x2{f0[0], f0[1]}; tw_[2] = u32x2{f0[2], f0[3]}; tw_[4] = u32x2{f1[0], f1[1]}; tw_[6] = u32x2{f1[2], f1[3]};
                    WAITL();
                    const LAS char* tr_ = tb + (4 * h + ((lane & 15) >> 2)) * 64 + tcol;
                    f0 = __builtin_bit_cast(u32x4, trfrag(tr_, 128)); f1 = __builtin_bit_cast(u32x4, trfrag(tr_ + 1024, 128));
                    WAITL();
                }
                st128(rs, dst + j * 262144, f0); st128(rs, dst + j * 262144 + 1024, f1);
            }
        }
#pragma unroll
        for (int i = 0; i < 7; ++i) *(LAS u32x4*)(lds + L_XS + w * 20480 + 8192 + i * 1024 + lane * 16) = wq[i];
        arrive(cnt + C_WQ, lane); waitc(cnt + C_WQ, 4u);

        bf16x8 q0, q1;
        {
            const LAS char* qr = (const LAS char*)lds + L_XS + 8192 + trow * 64 + tcol + ((56 * head) & 15);
            f32x16 qa = f32x16{}, qb = f32x16{};
#pragma unroll
            for (int g = 0; g < 4; ++g) {
                if (g == 2) publish(rs, cnt + C_Q, F_KV + (pr * 32 + isv * 16 + ti) * 128, tag, lane, 1);
                bf16x8 wf[7];
#pragma unroll
                for (int kk = 0; kk < 7; ++kk) {
                    wf[kk] = trfrag(qr + g * 20480 + kk * 1024, 64);
                    const int ks = 7 * g + kk;
                    if (ks >= 16) xf[ks] = *(const LAS bf16x8*)(xr + ((ks >> 2) % 5) * 4096 + (((2 * (ks & 3) + h) ^ xk) * 16));
                }
                SB();
#pragma unroll
                for (int kk = 0; kk < 7; ++kk) { if (kk & 1) qb = MFMA32(wf[kk], xf[7 * g + kk], qb); else qa = MFMA32(wf[kk], xf[7 * g + kk], qa); }
            }
            qa = (qa + qb) * QSCALE;
            u32x4 f0, f1;
#pragma unroll
            for (int e = 0; e < 4; ++e) { f0[e] = cvtpk(qa[2 * e], qa[2 * e + 1]); f1[e] = cvtpk(qa[8 + 2 * e], qa[9 + 2 * e]); }
            if (h) { f1[2] = 0u; f1[3] = 0u; }
            q0 = BC8(f0); q1 = BC8(f1);
        }
        LAS bf16x8* qs_ = (LAS bf16x8*)(lds + L_WP) + lane;
        qs_[w * 128] = q0;
        if (w == 0) { poll32(rs, F_KV + ((head >> 1) * 32 + (lane & 31)) * 128, tag); arrive(cnt + C_PK, lane); }
        else waitc(cnt + C_PK, 1u);
        const int kvo = WS_KV + head * 262144 + lane * 16;
        bf16x8 k0, k1, v0, v1, v1p, n0, n1, n2, n3;
        f32x16 sa, sb, o, oo[4];
        {
            n0 = BC8(ld128(rs, kvo)); n1 = BC8(ld128(rs, kvo + 1024));
            const int t0 = kvo + w * 4096;
            k0 = BC8(ld128(rs, t0)); k1 = BC8(ld128(rs, t0 + 1024)); v0 = BC8(ld128(rs, t0 + 2048)); v1 = BC8(ld128(rs, t0 + 3072));
            sa = MFMA32(n0, q0, f32x16{}); sa = MFMA32(n1, q1, sa);
            n0 = BC8(ld128(rs, t0 + 16384)); n1 = BC8(ld128(rs, t0 + 17408)); n2 = BC8(ld128(rs, t0 + 18432)); n3 = BC8(ld128(rs, t0 + 19456));
            q1 = set_ref(q1, xhalf_max(max16(sa)), h);
            qs_[w * 128 + 64] = q1;
            arrive(cnt + C_QS, lane); waitc(cnt + C_QS, 4u);
        }
        bf16x8 qa[4], qb[4];
#pragma unroll
        for (int i = 0; i < 4; ++i) { qa[i] = qs_[i * 128]; qb[i] = qs_[i * 128 + 64]; oo[i] = f32x16{}; }
        __builtin_amdgcn_s_setprio(0);
        sa = MFMA32(k0, qa[0], f32x16{}); sa = MFMA32(k1, qb[0], sa);
        v1p = v1;
        u32x4 p0, p1 = u32x4{};
        float e0 = 0.f, e1 = 0.f, e2 = 0.f, e3 = 0.f;
#define E4(SA, i) e0 = EX(SA[i]); e1 = EX(SA[i + 1]); e2 = EX(SA[i + 2]); e3 = EX(SA[i + 3]); SB();
#define STEPQ(SA, SN, K0, K1, QN, OP, VP, OC, V0) \
        SN = MFMA32(K0, qa[QN], f32x16{}); SB(); p1[2] = cvtpk(e0, e1); p1[3] = cvtpk(e2, e3); SB(); \
        E4(SA, 0) \
        oo[OP] = MFMA32(VP, BC8(p1), oo[OP]); SB(); p0[0] = cvtpk(e0, e1); p0[1] = cvtpk(e2, e3); SB(); \
        E4(SA, 4) \
        SN = MFMA32(K1, qb[QN], SN); SB(); p0[2] = cvtpk(e0, e1); p0[3] = cvtpk(e2, e3); SB(); \
        E4(SA, 8) \
        oo[OC] = MFMA32(V0, BC8(p0), oo[OC]); SB(); p1[0] = cvtpk(e0, e1); p1[1] = cvtpk(e2, e3); SB(); \
        E4(SA, 12)
#define TILE(K0, K1, V0, V1, VP, N0, N1, OFF) \
        STEPQ(sa, sb, K0, K1, 1, 3, VP, 0, V0) \
        VP = BC8(ld128(rs, (OFF) - 16384 + 3072)); SB(); \
        STEPQ(sb, sa, K0, K1, 2, 0, V1, 1, V0) \
        STEPQ(sa, sb, K0, K1, 3, 1, V1, 2, V0) \
        K0 = BC8(ld128(rs, (OFF))); K1 = BC8(ld128(rs, (OFF) + 1024)); SB(); \
        STEPQ(sb, sa, N0, N1, 0, 2, V1, 3, V0) \
        V0 = BC8(ld128(rs, (OFF) + 2048)); SB();
        n3 = v1p;
#pragma unroll 1
        for (int i = 0; i < 8; ++i) {
            const int off = kvo + (((2 * i + 2) * 4 + w) & 63) * 4096;
            const int offb = i == 7 ? off + 64 * 4096 : off;
            STEPQ(sa, sb, k0, k1, 1, 3, n3, 0, v0)
            n3 = BC8(ld128(rs, offb - 16384 + 3072)); SB();
            STEPQ(sb, sa, k0, k1, 2, 0, v1, 1, v0)
            STEPQ(sa, sb, k0, k1, 3, 1, v1, 2, v0)
            k0 = BC8(ld128(rs, off)); k1 = BC8(ld128(rs, off + 1024)); SB();
            STEPQ(sb, sa, n0, n1, 0, 2, v1, 3, v0)
            v0 = BC8(ld128(rs, off + 2048)); SB();
            TILE(n0, n1, n2, n3, v1, k0, k1, off + 16384)
        }
        p1[2] = cvtpk(e0, e1); p1[3] = cvtpk(e2, e3);
        oo[3] = MFMA32(n3, BC8(p1), oo[3]);
        __builtin_amdgcn_s_setprio(3);
        {
            LAS f32x4* ox = (LAS f32x4*)(lds + 16384) + lane;
#pragma unroll
            for (int i = 0; i < 4; ++i) {
                if (i != w) {
#pragma unroll
                    for (int e = 0; e < 4; ++e) ox[((w * 4 + i) * 4 + e) * 64] = f32x4{oo[i][4 * e], oo[i][4 * e + 1], oo[i][4 * e + 2], oo[i][4 * e + 3]};
                } else o = oo[i];
            }
            arrive(cnt + C_OX, lane); waitc(cnt + C_OX, 4u);
#pragma unroll
            for (int i = 1; i < 4; ++i) {
#pragma unroll
                for (int e = 0; e < 4; ++e) { const f32x4 v = ox[((((w + i) & 3) * 4 + w) * 4 + e) * 64]; o[4 * e] += v[0]; o[4 * e + 1] += v[1]; o[4 * e + 2] += v[2]; o[4 * e + 3] += v[3]; }
            }
        }
        float l = hi_half(o[12]);
        float chk = 0.f;
#pragma unroll
        for (int e = 0; e < 16; ++e) chk += o[e] * 0.f;
        if (__builtin_amdgcn_ballot_w64(!(l > 0.25f && l < 1.0e18f && chk == 0.f)) != 0ull) {
            const int g = WS_KV + head * 262144 + lane * 16;
            q1 = set_ref(q1, 0.f, h);
            float m = -INFINITY;
#pragma unroll 1
            for (int t = 0; t < 64; ++t) {
                f32x16 s = MFMA32(BC8(ld128(rs, g + t * 4096)), q0, f32x16{});
                s = MFMA32(BC8(ld128(rs, g + t * 4096 + 1024)), q1, s);
                m = __builtin_fmaxf(m, max16(s));
            }
            q1 = set_ref(q1, xhalf_max(m), h);
            o = f32x16{};
#pragma unroll 1
            for (int t = 0; t < 64; ++t) {
                f32x16 s = MFMA32(BC8(ld128(rs, g + t * 4096)), q0, f32x16{});
                s = MFMA32(BC8(ld128(rs, g + t * 4096 + 1024)), q1, s);
#pragma unroll
                for (int e = 0; e < 4; ++e) { p0[e] = cvtpk(EX(s[2 * e]), EX(s[2 * e + 1])); p1[e] = cvtpk(EX(s[8 + 2 * e]), EX(s[9 + 2 * e])); }
                o = MFMA32(BC8(ld128(rs, g + t * 4096 + 2048)), BC8(p0), o);
                o = MFMA32(BC8(ld128(rs, g + t * 4096 + 3072)), BC8(p1), o);
            }
            l = hi_half(o[12]);
        }
        const float inv = 1.0f / l;
#pragma unroll
        for (int e = 0; e < 4; ++e) { p0[e] = cvtpk(o[2 * e] * inv, o[2 * e + 1] * inv); p1[e] = cvtpk(o[8 + 2 * e] * inv, o[9 + 2 * e] * inv); }
        {
            const int dst = WS_OF + ((((ti * 4 + w) * 16 + head) * 2) * 64 + lane) * 16;
            waitc(cnt + C_R0, 4u);
            st128(rs, dst, p0); st128(rs, dst + 1024, p1);
        }
        publish(rs, cnt + C_O, F_O + (ti * 16 + head) * 128, tag, lane, 1);

    } else {
        __builtin_amdgcn_s_setprio(2);
        const int b = bid, t = tid - 256;
        unsigned wv[8];
        LAS cf* fb = (LAS cf*)(lds + L_F);
        LAS cf* f = fb + w * 7 * RSTR;
        const int l2 = lane < 56 ? lane : 55;
        const int n2 = 2 * t, pos = (n2 >> 6) * RSTR + (n2 & 63);
        const float rr = 0.70710678118654752f;
        const int s2 = l2 >> 3, r0 = l2 & 7, m1 = l2 / 7, s3 = l2 - 7 * m1;
        const int j = 8 * w + (lane >> 3), j0 = (lane >> 3) & 3, d0 = 2 * w + (lane >> 5);
        cf a[8], t448[7], t64[8], t256[8], t32[8], tb1, tb2, tb3, tb4;
        {
            unsigned xv[8];
            const int tc = t < 224 ? t : 223;
#pragma unroll
            for (int i = 0; i < 8; ++i) xv[i] = *(const unsigned*)(bx + (size_t)(256 * i + b) * 448 + 2 * tc);
            {
                const char* wsrc = (const char*)wqkv + 896 * (1 + isv) + 112 * pr;
                const char* xsrc = (const char*)x + (size_t)(ti * 128 + w * 32 + (lane >> 3)) * 896;
                const int pa = 2 * w, pb = w < 3 ? 2 * w + 1 : 6;
                const int ca = pa * 64 + lane, cb_ = pb * 64 + lane;
                const int oa = (ca / 7) * 2688 + (ca % 7) * 16, ob = (cb_ / 7) * 2688 + (cb_ % 7) * 16;
                const int x0 = ((lane & 7) ^ (lane >> 4)) * 16, x1 = ((lane & 7) ^ (4 + (lane >> 4))) * 16;
                auto ldc = [&](int c) __attribute__((always_inline)) {
                    glds16(wsrc + c * (64 * 2688) + oa, lds0 + L_WP + (c * 7 + pa) * 1024);
                    glds16(wsrc + c * (64 * 2688) + ob, lds0 + L_WP + (c * 7 + pb) * 1024);
                    if (c < 5) {
#pragma unroll
                        for (int i = 0; i < 4; ++i) glds16(xsrc + i * (8 * 896) + c * 128 + ((i & 1) ? x1 : x0), lds0 + L_XS + w * 20480 + c * 4096 + i * 1024);
                    }
                };
                ldc(0); ldc(1); ldc(2); ldc(3); ldc(5); ldc(6);
                WAITV(16); arrive(cnt + C_M + 1, lane); ldc(4);
                WAITV(10); arrive(cnt + C_M + 3, lane);
                WAITV(0); arrive(cnt + C_M + 6, lane);
            }
            const int item = bid * 112 + (t < 112 ? t : 111), l = item & 63, fr = item >> 6, hd = fr & 15, cb = fr >> 4;
            const int g = l >> 4, dd = 16 * (g >> 1) + 4 * (g & 1);
            const unsigned short* src = wo + (size_t)(hd * 28) * 448 + cb * 16 + (l & 15);
            if (w < 2) {
#pragma unroll
                for (int e = 0; e < 8; ++e) { const int d = dd + 8 * (e >> 2) + (e & 3); wv[e] = src[(d < 28 ? d : 0) * 448]; }
            }
#pragma unroll
            for (int i = 1; i < 8; ++i) { if (i < 7) t448[i] = tw(lane * i, 1.f / 448); t64[i] = tw(r0 * i, 1.f / 64); t256[i] = tw(j * i, 1.f / 256); t32[i] = tw(j0 * i, 1.f / 32); }
            tb1 = tw(b, 1.f / 2048); tb2 = tw(2 * b, 1.f / 2048); tb3 = tw(3 * b, 1.f / 2048); tb4 = tw(b, 1.f / 512);
#pragma unroll
            for (int i = 1; i < 8; ++i) { if (i < 7) asm volatile("" : "+v"(t448[i])); asm volatile("" : "+v"(t64[i]), "+v"(t256[i]), "+v"(t32[i])); }
            asm volatile("" : "+v"(tb1), "+v"(tb2), "+v"(tb3), "+v"(tb4));
            if (t < 224) {
                cf v[8];
#pragma unroll
                for (int i = 0; i < 8; ++i) v[i] = cf{bflo(xv[i]), bfhi(xv[i])};
                cf p0 = v[0] + v[4], m0 = v[0] - v[4], p1 = v[1] + v[5], n1 = v[1] - v[5];
                cf p2 = v[2] + v[6], m2 = v[2] - v[6], p3 = v[3] + v[7], m3 = v[3] - v[7];
                cf e0 = p0 + p2, e1 = p1 + p3;
                cf z0 = e0 + e1, z4 = e0 - e1;
                cf z2r = p0 - p2, z2i = p3 - p1;
                cf qa = (n1 - m3) * rr, qb = (n1 + m3) * rr;
                cf z1r = m0 + qa, z1i = -m2 - qb, z3r = m0 - qa, z3i = m2 - qb;
                const cf w1 = tb1, w2 = tb2, w3 = tb3;
                cf a0, a1;
                *(LAS f32x4*)(fb + pos) = f32x4{z0.x, z4.x, z0.y, z4.y};
                a0 = cmul<-1>(cf{z1r.x, z1i.x}, w1); a1 = cmul<-1>(cf{z1r.y, z1i.y}, w1);
                *(LAS f32x4*)(fb + 7 * RSTR + pos) = f32x4{a0.x, a0.y, a1.x, a1.y};
                a0 = cmul<-1>(cf{z2r.x, z2i.x}, w2); a1 = cmul<-1>(cf{z2r.y, z2i.y}, w2);
                *(LAS f32x4*)(fb + 14 * RSTR + pos) = f32x4{a0.x, a0.y, a1.x, a1.y};
                a0 = cmul<-1>(cf{z3r.x, z3i.x}, w3); a1 = cmul<-1>(cf{z3r.y, z3i.y}, w3);
                *(LAS f32x4*)(fb + 21 * RSTR + pos) = f32x4{a0.x, a0.y, a1.x, a1.y};
            }
            arrive(cnt + C_B0, lane);
            if (w >= 2 && lane < 40) {
                int e = (8 * (8 * (bid >> 3) + lane / 5) + xcd) * 225 + 7 * xcd + (lane % 5) * 64;
                e = e < 2048 * 225 ? e : 2048 * 225 - 1;
                wv[0] = (w == 2 ? fwr : fwi)[e];
            }
            waitc(cnt + C_B0, 4u);
        }
#pragma unroll
        for (int p = 0; p < 7; ++p) a[p] = f[p * RSTR + lane];
        dft7<-1>(a);
#pragma unroll
        for (int s = 1; s < 7; ++s) a[s] = cmul<-1>(a[s], t448[s]);
#pragma unroll
        for (int s = 0; s < 7; ++s) f[s * RSTR + lane] = a[s];
        WAITL();
#pragma unroll
        for (int i = 0; i < 8; ++i) a[i] = f[s2 * RSTR + 8 * i + r0];
        dft8<-1>(a);

#pragma unroll
        for (int i = 1; i < 8; ++i) a[i] = cmul<-1>(a[i], t64[i]);
        if (lane < 56) {
#pragma unroll
            for (int i = 0; i < 8; ++i) f[s2 * RSTR + 8 * i + r0] = a[i];
        }
        WAITL();
#pragma unroll
        for (int i = 0; i < 8; ++i) a[i] = f[s3 * RSTR + 8 * m1 + i];
        dft8<-1>(a);
        {
            LAS cf* y = (LAS cf*)(lds + L_YA);
            if (w != 0) {
                if (lane < 56) {
#pragma unroll
                    for (int i = 0; i < 4; ++i) y[w * 225 + lane + 56 * i] = a[i];
#pragma unroll
                    for (int i = 4; i < 8; ++i) y[(8 - w) * 225 + 448 - lane - 56 * i] = cconj(a[i]);
                    if (lane == 0) { y[w * 225 + 224] = a[4]; y[(8 - w) * 225] = cconj(a[0]); }
                }
            } else {
                WAITL();
                if (lane < 56) {
#pragma unroll
                    for (int i = 0; i < 8; ++i) f[lane + 56 * i] = a[i];
                }
                if (lane == 0) f[448] = a[0];
                WAITL();
                const cf w4 = cf{tb4.y, -tb4.x} * 0.5f;
#pragma unroll
                for (int i = 0; i < 4; ++i) {
                    const int k = lane + 64 * i;
                    if (k <= 224) {
                        const cf u = f[k], v = cconj(f[448 - k]);
                        y[k] = (u + v) * 0.5f;
                        y[4 * 225 + k] = cmul<-1>(u - v, w4);
                    }
                }
            }
            arrive(cnt + C_B1, lane); waitc(cnt + C_B1, 4u);
#pragma unroll
            for (int i = 0; i < 4; ++i) { const int e = t + 256 * i; if (e < 900) st128(rs, WS_Y1 + b * (NQ * 8) + e * 16, ((LAS u32x4*)y)[e]); }
            const bool pad = ((bid * 112 + t) & 48) == 48;
            if (t < 112) st128(rs, WS_WO + (bid * 112 + t) * 16, u32x4{wv[0] | (wv[1] << 16), wv[2] | (wv[3] << 16), pad ? 0u : wv[4] | (wv[5] << 16), pad ? 0u : wv[6] | (wv[7] << 16)});
        }
        asm volatile("s_waitcnt vmcnt(0)\n\tv_readfirstlane_b32 %0, %1" : "=s"(tag) : "v"(genv) : "memory");
        asm volatile("" :: "v"(wv[0]));
        tag += 1u;
        publish(rs, cnt + C_B3, F_Y1 + bid * 4, tag, lane, 8);
        waitc(cnt + C_PK, 1u);
        {
            const int grp = (bid & 7) * 29 + (bid >> 3);
            const bool act = (bid >> 3) < 29 && grp < 225;
            const int col = lane & 7, q = (act ? grp : 0) * 8 + col, c = q / 225, k2 = q - c * 225, cs = c > 4 ? c - 8 : c;
            {
                const int dst = WS_OF + ((((ti * 4 + w) * 16 + head) * 2) * 64 + lane) * 16;
                st128(rs, dst, u32x4{}); st128(rs, dst + 1024, u32x4{});
            }
            unsigned fr_[8], fi_[8];
#pragma unroll
            for (int i = 0; i < 8; ++i) {
                const int d = d0 + 8 * (j0 + 4 * (i >> 2)) + 64 * (i & 3), k1 = (cs + 8 * d) & 2047;
                fr_[i] = fwr[k1 * 225 + k2]; fi_[i] = fwi[k1 * 225 + k2];
                __builtin_amdgcn_s_sleep(3); FENCE();
            }
            if (w == 0) {
                poll128(rs, F_Y1 + xcd * FREP + lane * 16, tag);
                arrive(cnt + C_B2, lane);
            } else waitc(cnt + C_B2, 1u);
            if (t < 112) { wv[0] = ld32(rs, WS_WO + ((bid >> 3) * 112 + t) * 128); asm volatile("" :: "v"(wv[0])); }
            LAS cf* mb = fb;
#pragma unroll
            for (int i = 0; i < 8; ++i) a[i] = ld64(rs, WS_Y1 + ((32 * i + j) * NQ + q) * 8);
            if (act) {
                dft8<-1>(a);
#pragma unroll
                for (int i = 1; i < 8; ++i) a[i] = cmul<-1>(a[i], t256[i]);
#pragma unroll
                for (int i = 0; i < 8; ++i) mb[(i * 32 + j) * 8 + col] = a[i];
                arrive(cnt + C_B4, lane); waitc(cnt + C_B4, 4u);
#pragma unroll
                for (int i = 0; i < 8; ++i) a[i] = mb[(d0 * 32 + 4 * i + j0) * 8 + col];
                dft8<-1>(a);
#pragma unroll
                for (int i = 1; i < 8; ++i) a[i] = cmul<-1>(a[i], t32[i]);
#pragma unroll
                for (int i = 0; i < 8; ++i) mb[(d0 * 32 + 4 * i + j0) * 8 + col] = a[i];
                WAITL();
#pragma unroll
                for (int i = 0; i < 8; ++i) a[i] = mb[(d0 * 32 + 4 * (j0 + 4 * (i >> 2)) + (i & 3)) * 8 + col];
                dft4<-1>(a[0], a[1], a[2], a[3]); dft4<-1>(a[4], a[5], a[6], a[7]);
#pragma unroll
                for (int i = 0; i < 8; ++i) a[i] = cmul<1>(a[i], cf{bflo(fi_[i]), bflo(fr_[i])});
                dft4<1>(a[0], a[1], a[2], a[3]); dft4<1>(a[4], a[5], a[6], a[7]);
#pragma unroll
                for (int i = 0; i < 8; ++i) mb[(d0 * 32 + 4 * (j0 + 4 * (i >> 2)) + (i & 3)) * 8 + col] = a[i];
                WAITL();
#pragma unroll
                for (int i = 0; i < 8; ++i) a[i] = mb[(d0 * 32 + 4 * i + j0) * 8 + col];
#pragma unroll
                for (int i = 1; i < 8; ++i) a[i] = cmul<1>(a[i], t32[i]);
                dft8<1>(a);
#pragma unroll
                for (int i = 0; i < 8; ++i) mb[(d0 * 32 + 4 * i + j0) * 8 + col] = a[i];
                arrive(cnt + C_B5, lane); waitc(cnt + C_B5, 4u);
#pragma unroll
                for (int i = 0; i < 8; ++i) a[i] = mb[(i * 32 + j) * 8 + col];
#pragma unroll
                for (int i = 1; i < 8; ++i) a[i] = cmul<1>(a[i], t256[i]);
                dft8<1>(a);
#pragma unroll
                for (int i = 0; i < 8; ++i) st64(rs, WS_Y2 + ((32 * i + j) * NQ + q) * 8, a[i]);
            }
        }
        publish(rs, cnt + C_R0, F_Y2 + bid * 4, tag, lane, 8);
        bf16x8 wa[16], wb[16];
        const int cb0 = cq * 7 + w, cb1 = cq * 7 + (w < 3 ? w + 4 : 6);
#pragma unroll
        for (int i = 0; i < 16; ++i) { wa[i] = BC8(ld128(rs, WS_WO + ((cb0 * 16 + i) * 64 + lane) * 16)); wb[i] = BC8(ld128(rs, WS_WO + ((cb1 * 16 + i) * 64 + lane) * 16)); __builtin_amdgcn_s_sleep(5); FENCE(); }
        waitc(cnt + C_R0, 4u);
        if (w == 0) {
            poll128(rs, F_Y2 + xcd * FREP + lane * 16, tag);
            arrive(cnt + C_PY, lane);
            if (bid == 0 && lane == 0) st32(rs, F_GEN, tag);
        } else waitc(cnt + C_PY, 1u);
        {
            const int y = WS_Y2 + b * (NQ * 8);
            cf z[8];
            const int ya = y + (w * 225) * 8, yb = y + ((w ? 8 - w : 0) * 225) * 8;
#pragma unroll
            for (int i = 0; i < 8; ++i) a[i] = ld64(rs, i < 4 ? ya + (l2 + 56 * i) * 8 : yb + (448 - l2 - 56 * i) * 8);
            if (w) { z[0] = ld64(rs, yb); z[4] = ld64(rs, ya + 224 * 8); }
            else {
#pragma unroll
                for (int i = 0; i < 8; ++i) z[i] = ld64(rs, y + (900 + (i < 4 ? l2 + 56 * i : 448 - l2 - 56 * i)) * 8);
            }
            waitc(cnt + C_OX, 1u);
#pragma unroll
            for (int i = 4; i < 8; ++i) a[i] = cconj(a[i]);
            if (w) { if (lane == 0) { a[0] = (a[0] + cconj(z[0])) * 0.5f; a[4] = (a[4] + z[4]) * 0.5f; } }
            else {
                const cf w4 = tb4;
#pragma unroll
                for (int i = 0; i < 8; ++i) {
                    cf z4 = cmul<1>(z[i], w4);
                    if (i >= 4) z4 = cconj(z4);
                    if ((i & 3) == 0 && lane == 0) { a[i].y = 0.f; z4.y = 0.f; }
                    a[i] = addrot<1>(a[i], z4);
                }
            }
        }
        dft8<1>(a);
        if (lane < 56) {
#pragma unroll
            for (int i = 0; i < 8; ++i) f[s3 * RSTR + 8 * m1 + i] = a[i];
        }
        WAITL();
#pragma unroll
        for (int i = 0; i < 8; ++i) a[i] = f[s2 * RSTR + 8 * i + r0];
#pragma unroll
        for (int i = 1; i < 8; ++i) a[i] = cmul<1>(a[i], t64[i]);
        dft8<1>(a);
        if (lane < 56) {
#pragma unroll
            for (int i = 0; i < 8; ++i) f[s2 * RSTR + 8 * i + r0] = a[i];
        }
        WAITL();
#pragma unroll
        for (int s = 0; s < 7; ++s) a[s] = f[s * RSTR + lane];
#pragma unroll
        for (int s = 1; s < 7; ++s) a[s] = cmul<1>(a[s], t448[s]);
        dft7<1>(a);
#pragma unroll
        for (int p = 0; p < 7; ++p) f[p * RSTR + lane] = a[p];
        arrive(cnt + C_B6, lane); waitc(cnt + C_B6, 4u);
        if (t < 224) {
            const float sc = 1.0f / (2048.0f * 448.0f), sc2 = 2.f * sc;
            const f32x4 u0 = *(LAS f32x4*)(fb + pos), u1 = *(LAS f32x4*)(fb + 7 * RSTR + pos), u2 = *(LAS f32x4*)(fb + 14 * RSTR + pos), u3 = *(LAS f32x4*)(fb + 21 * RSTR + pos);
            const cf w1 = tb1 * sc2, w2 = tb2 * sc2, w3 = tb3 * sc2;
            const cf z0 = cf{u0[0], u0[2]} * sc, z4 = cf{u0[1], u0[3]} * sc;
            cf a0 = cmul<1>(cf{u1[0], u1[1]}, w1), a1 = cmul<1>(cf{u1[2], u1[3]}, w1);
            const cf z1r = cf{a0.x, a1.x}, z1i = cf{a0.y, a1.y};
            a0 = cmul<1>(cf{u2[0], u2[1]}, w2); a1 = cmul<1>(cf{u2[2], u2[3]}, w2);
            const cf z2r = cf{a0.x, a1.x}, z2i = cf{a0.y, a1.y};
            a0 = cmul<1>(cf{u3[0], u3[1]}, w3); a1 = cmul<1>(cf{u3[2], u3[3]}, w3);
            const cf z3r = cf{a0.x, a1.x}, z3i = cf{a0.y, a1.y};
            const cf e0 = z0 + z4, e1 = z0 - z4;
            const cf p0 = e0 + z2r, p2 = e0 - z2r, p1 = e1 - z2i, p3 = e1 + z2i;
            const cf m0 = z1r + z3r, m2 = z3i - z1i;
            const cf qa = (z1r - z3r) * rr, qb = (z1i + z3i) * rr;
            const cf mm1 = qa - qb, m3 = -qa - qb;
            const cf yv[8] = {p0 + m0, p1 + mm1, p2 + m2, p3 + m3, p0 - m0, p1 - mm1, p2 - m2, p3 - m3};
#pragma unroll
            for (int i = 0; i < 8; ++i) *(unsigned*)(out + (size_t)(256 * i + b) * 896 + 448 + n2) = cvtpk(yv[i].x, yv[i].y);
        }
        waitc(cnt + C_OX, 4u);
        __builtin_amdgcn_s_setprio(3);
        if (w == 0) { poll32(rs, F_O + ((otile >> 2) * 16 + (lane & 15)) * 128, tag); arrive(cnt + C_PO, lane); }
        else waitc(cnt + C_PO, 1u);
        u32x4 ov[8];
#pragma unroll
        for (int i = 0; i < 8; ++i) ov[i] = ld128(rs, WS_OF + (otile * 2048 + (i * 4 + w) * 64 + lane) * 16);
#pragma unroll
        for (int i = 0; i < 8; ++i) ((LAS u32x4*)(lds + 98304))[(i * 4 + w) * 64 + lane] = ov[i];
        arrive(cnt + C_OS, lane); waitc(cnt + C_OS, 4u);
        {
            const int g = lane >> 4;
            const LAS bf16x8* op = (const LAS bf16x8*)(lds + 98304) + (g >> 1) * 64 + (g & 1) * 32 + (lane & 15);
            f32x4 c0 = f32x4{}, c1 = f32x4{}, c2 = f32x4{}, c3 = f32x4{};
#pragma unroll
            for (int i = 0; i < 16; ++i) {
                const bf16x8 a0 = op[i * 128], a1 = op[i * 128 + 16];
                c0 = MFMA16(wa[i], a0, c0); c1 = MFMA16(wa[i], a1, c1); c2 = MFMA16(wb[i], a0, c2); c3 = MFMA16(wb[i], a1, c3);
            }
            unsigned short* dst = out + (size_t)(otile * 32 + (lane & 15)) * 896 + g * 4;
            *(u32x2*)(dst + cb0 * 16) = u32x2{cvtpk(c0[0], c0[1]), cvtpk(c0[2], c0[3])};
            *(u32x2*)(dst + cb0 * 16 + 16 * 896) = u32x2{cvtpk(c1[0], c1[1]), cvtpk(c1[2], c1[3])};
            if (w < 3) {
                *(u32x2*)(dst + cb1 * 16) = u32x2{cvtpk(c2[0], c2[1]), cvtpk(c2[2], c2[3])};
                *(u32x2*)(dst + cb1 * 16 + 16 * 896) = u32x2{cvtpk(c3[0], c3[1]), cvtpk(c3[2], c3[3])};
            }
        }
    }
}

extern "C" void kernel_launch(void* const* d_in, const int* in_sizes, int n_in, void* d_out, int out_size, void* d_ws,
                              size_t ws_size, hipStream_t stream) {
    (void)hipFuncSetAttribute((const void*)fused_kernel, hipFuncAttributeMaxDynamicSharedMemorySize, L_END);
    fused_kernel<<<256, 512, L_END, stream>>>((const unsigned short*)d_in[0], (const unsigned short*)d_in[1], (const unsigned short*)d_in[2],
                                              (const unsigned short*)d_in[3], (const unsigned short*)d_in[4], (const unsigned short*)d_in[5],
                                              (unsigned short*)d_out, (char*)d_ws);
}
```

```cpp
#include <hip/hip_runtime.h>
#include <hip/hip_bf16.h>
#include <stdint.h>

typedef __attribute__((ext_vector_type(8))) short bf16x8;
typedef __attribute__((ext_vector_type(4))) short s16x4;
typedef __attribute__((ext_vector_type(16))) float f32x16;
typedef __attribute__((ext_vector_type(4))) float f32x4;
typedef __attribute__((ext_vector_type(4))) unsigned u32x4;
typedef __attribute__((ext_vector_type(2))) unsigned u32x2;
typedef __attribute__((ext_vector_type(2))) float cf;
#define LAS __attribute__((address_space(3)))
#if defined(__HIP_DEVICE_COMPILE__)
#define NOPK __attribute__((target("no-packed-fp32-ops")))
#else
#define NOPK
#endif
#define DEVI __device__ __forceinline__
#define MFMA32(a, b, c) __builtin_amdgcn_mfma_f32_32x32x16_bf16(a, b, c, 0, 0, 0)
#define MFMA16(a, b, c) __builtin_amdgcn_mfma_f32_16x16x32_bf16(a, b, c, 0, 0, 0)

constexpr int NQ = 1800;
constexpr int F_GEN = 0, F_KV = 256, F_O = F_KV + 256 * 128, F_Y1 = F_O + 256 * 128, FREP = 1024 + 256, F_Y2 = F_Y1 + 8 * FREP;
constexpr int WS_KV = 131072;
constexpr int WS_WO = WS_KV + 16 * 64 * 4 * 1024;
constexpr int WS_OF = WS_WO + 28 * 16 * 1024;
constexpr int WS_Y1 = WS_OF + 64 * 16 * 2 * 1024;
constexpr int WS_Y2 = WS_Y1;
constexpr int WS_END = WS_Y1 + 256 * NQ * 8;
constexpr float QSCALE = 0.18898223650461362f * 1.4426950408889634f;

typedef __attribute__((ext_vector_type(2))) __bf16 bf16x2_t;
DEVI unsigned cvtpk(float lo, float hi) { return __builtin_bit_cast(unsigned, __builtin_convertvector(cf{lo, hi}, bf16x2_t)); }
DEVI float bflo(unsigned u) { return __uint_as_float(u << 16); }
DEVI float bfhi(unsigned u) { return __uint_as_float(u & 0xffff0000u); }
DEVI void glds16(const void* g, unsigned lds_base) {
    unsigned sv;
    asm volatile("s_mov_b32 %0, m0\n\ts_mov_b32 m0, %2\n\ts_nop 0\n\tglobal_load_lds_dwordx4 %1, off\n\ts_mov_b32 m0, %0"
                 : "=&s"(sv) : "v"(g), "s"(__builtin_amdgcn_readfirstlane(lds_base)) : "memory");
}
#define WAITV(N) asm volatile("s_waitcnt vmcnt(" #N ")" ::: "memory")
#define WAITL() asm volatile("s_waitcnt lgkmcnt(0)" ::: "memory")
#define BAR() asm volatile("s_waitcnt lgkmcnt(0)\n\ts_barrier" ::: "memory")

template <int DIR> DEVI cf cmul(cf a, cf w) {
    return DIR > 0 ? cf{__builtin_fmaf(a.x, w.y, -a.y * w.x), __builtin_fmaf(a.y, w.y, a.x * w.x)}
                   : cf{__builtin_fmaf(a.x, w.y, a.y * w.x), __builtin_fmaf(a.y, w.y, -a.x * w.x)};
}
template <int DIR> DEVI cf addrot(cf t, cf x) { return DIR > 0 ? cf{t.x - x.y, t.y + x.x} : cf{t.x + x.y, t.y - x.x}; }
DEVI cf cconj(cf a) { return cf{a.x, -a.y}; }
DEVI cf tw(int num, float inv_den) {
    const float x = (float)num * inv_den;
    return cf{__builtin_amdgcn_sinf(x), __builtin_amdgcn_cosf(x)};
}
template <int DIR> DEVI void dft4(cf& a0, cf& a1, cf& a2, cf& a3) {
    cf t0 = a0 + a2, t1 = a0 - a2, t2 = a1 + a3, t3 = a1 - a3;
    a0 = t0 + t2; a2 = t0 - t2; a1 = addrot<DIR>(t1, t3); a3 = addrot<-DIR>(t1, t3);
}
template <int DIR> DEVI void dft8(cf* a) {
    cf e0 = a[0], e1 = a[2], e2 = a[4], e3 = a[6], o0 = a[1], o1 = a[3], o2 = a[5], o3 = a[7];
    dft4<DIR>(e0, e1, e2, e3); dft4<DIR>(o0, o1, o2, o3);
    const float r = 0.70710678118654752f;
    o1 = addrot<DIR>(o1, o1); o3 = addrot<-DIR>(o3, o3);
    a[0] = e0 + o0; a[4] = e0 - o0; a[1] = e1 + o1 * r; a[5] = e1 - o1 * r;
    a[2] = addrot<DIR>(e2, o2); a[6] = addrot<-DIR>(e2, o2); a[3] = e3 - o3 * r; a[7] = e3 + o3 * r;
}
template <int DIR> DEVI void dft7(cf* a) {
    const float c1 = 0.62348980185873353f, c2 = -0.22252093395631440f, c3 = -0.90096886790241913f;
    const float s1 = 0.78183148246802981f, s2 = 0.97492791218182361f, s3 = 0.43388373911755812f;
    cf t1 = a[1] + a[6], t2 = a[2] + a[5], t3 = a[3] + a[4], u1 = a[1] - a[6], u2 = a[2] - a[5], u3 = a[3] - a[4];
    cf A1 = a[0] + t1 * c1 + t2 * c2 + t3 * c3, A2 = a[0] + t1 * c2 + t2 * c3 + t3 * c1, A3 = a[0] + t1 * c3 + t2 * c1 + t3 * c2;
    cf B1 = u1 * s1 + u2 * s2 + u3 * s3, B2 = u1 * s2 - u2 * s3 - u3 * s1, B3 = u1 * s3 - u2 * s1 + u3 * s2;
    a[0] = a[0] + t1 + t2 + t3;
    a[1] = addrot<DIR>(A1, B1); a[6] = addrot<-DIR>(A1, B1); a[2] = addrot<DIR>(A2, B2); a[5] = addrot<-DIR>(A2, B2);
    a[3] = addrot<DIR>(A3, B3); a[4] = addrot<-DIR>(A3, B3);
}
constexpr int RSTR = 72;
constexpr int RFB = 7 * RSTR * 8;

typedef __amdgpu_buffer_rsrc_t rsrc_t;
DEVI void st128(rsrc_t r, int off, u32x4 v) { __builtin_amdgcn_raw_buffer_store_b128(v, r, off, 0, 16); }
DEVI void st64(rsrc_t r, int off, cf v) { __builtin_amdgcn_raw_buffer_store_b64(__builtin_bit_cast(u32x2, v), r, off, 0, 16); }
DEVI void st32(rsrc_t r, int off, unsigned v) { __builtin_amdgcn_raw_buffer_store_b32(v, r, off, 0, 16); }
DEVI u32x4 ld128(rsrc_t r, int off) { return __builtin_amdgcn_raw_buffer_load_b128(r, off, 0, 16); }
DEVI cf ld64(rsrc_t r, int off) { return __builtin_bit_cast(cf, __builtin_amdgcn_raw_buffer_load_b64(r, off, 0, 16)); }
DEVI unsigned ld32(rsrc_t r, int off) { return __builtin_amdgcn_raw_buffer_load_b32(r, off, 0, 16); }
#define FENCE() asm volatile("" ::: "memory")
constexpr int SPIN_CAP = 1 << 18;
DEVI void arrive(LAS unsigned* c, int) {
    asm volatile("s_waitcnt lgkmcnt(0)\n\ts_mov_b64 exec, 1\n\tds_add_u32 %0, %1\n\ts_mov_b64 exec, -1" :: "v"(c), "v"(1u) : "memory");
}
DEVI unsigned arrive_rtn(LAS unsigned* c) {
    unsigned v, r;
    asm volatile("s_waitcnt lgkmcnt(0)\n\ts_mov_b64 exec, 1\n\tds_add_rtn_u32 %0, %2, %3\n\ts_waitcnt lgkmcnt(0)\n\tv_readfirstlane_b32 %1, %0\n\ts_mov_b64 exec, -1"
                 : "=&v"(v), "=s"(r) : "v"(c), "v"(1u) : "memory");
    return r;
}
DEVI void waitc(LAS unsigned* c, unsigned n) {
    unsigned v, sv, it;
    asm volatile("s_mov_b32 %2, 0x200000\n"
                 "1:\n\tds_read_b32 %0, %3\n\ts_waitcnt lgkmcnt(0)\n\tv_readfirstlane_b32 %1, %0\n\ts_cmp_ge_u32 %1, %4\n\ts_cbranch_scc1 2f\n\t"
                 "s_sleep 3\n\ts_sub_u32 %2, %2, 1\n\ts_cbranch_scc0 1b\n2:"
                 : "=&v"(v), "=&s"(sv), "=&s"(it) : "v"(c), "s"(n) : "memory", "scc");
}
DEVI void publish(rsrc_t r, LAS unsigned* c, int flag_off, unsigned tag, int lane, int nrep) {
    WAITV(0);
    if (arrive_rtn(c) == 3u && lane < nrep) st32(r, flag_off + lane * FREP, tag);
}
DEVI void poll32(rsrc_t r, int off, unsigned tag) {
#pragma unroll 1
    for (int i = 0; i < SPIN_CAP; ++i) { if (__builtin_amdgcn_ballot_w64(ld32(r, off) != tag) == 0ull) break; __builtin_amdgcn_s_sleep(2); FENCE(); }
    FENCE();
}
DEVI void poll128(rsrc_t r, int off, unsigned tag) {
#pragma unroll 1
    for (int i = 0; i < SPIN_CAP; ++i) {
        const u32x4 v = ld128(r, off);
        if (__builtin_amdgcn_ballot_w64(!(v[0] == tag && v[1] == tag && v[2] == tag && v[3] == tag)) == 0ull) break;
        __builtin_amdgcn_s_sleep(10); FENCE();
    }
    FENCE();
}

DEVI float xhalf_max(float m) {
    auto rr = __builtin_amdgcn_permlane32_swap(__float_as_uint(m), __float_as_uint(m), false, false);
    return __builtin_fmaxf(__uint_as_float(rr[0]), __uint_as_float(rr[1]));
}
DEVI float hi_half(float m) {
    auto rr = __builtin_amdgcn_permlane32_swap(__float_as_uint(m), __float_as_uint(m), false, false);
    return __uint_as_float(rr[1]);
}
DEVI float max16(const f32x16& s) {
    float m = __builtin_fmaxf(s[0], s[1]);
#pragma unroll
    for (int i = 2; i < 16; ++i) m = __builtin_fmaxf(m, s[i]);
    return m;
}
DEVI bf16x8 set_ref(bf16x8 q1, float m, int h) {
    const float nm = -m;
    const unsigned b0 = __float_as_uint(nm) & 0xffff0000u; const float r1 = nm - __uint_as_float(b0);
    const unsigned b1 = __float_as_uint(r1) & 0xffff0000u; const float r2 = r1 - __uint_as_float(b1);
    const unsigned b2 = __float_as_uint(r2) & 0xffff0000u;
    u32x4 u = __builtin_bit_cast(u32x4, q1);
    if (h) { u[2] = (b0 >> 16) | b1; u[3] = b2 >> 16; }
    return __builtin_bit_cast(bf16x8, u);
}
DEVI bf16x8 trfrag(const LAS char* p, int rowstride) {
    s16x4 lo = __builtin_amdgcn_ds_read_tr16_b64_v4i16((LAS s16x4*)p);
    s16x4 hi = __builtin_amdgcn_ds_read_tr16_b64_v4i16((LAS s16x4*)(p + 4 * rowstride));
    return __builtin_shufflevector(lo, hi, 0, 1, 2, 3, 4, 5, 6, 7);
}
#define SB() __builtin_amdgcn_sched_barrier(0)
#define EX(x) __builtin_amdgcn_exp2f(x)
#define BC8(x) __builtin_bit_cast(bf16x8, x)

#define HX 2
constexpr int L_WP = 0, L_XS = 50240, L_F = L_XS + 4 * 20480, L_YA = L_F + 16384, L_C = L_YA + 14400, L_END = L_C + 256;
enum { C_Q = 0, C_PK, C_PO, C_PY, C_WQ, C_R0, C_O, C_OS, C_QS, C_OX, C_B0, C_B1, C_B2, C_B3, C_B4, C_B5, C_B6, C_M = 40 };

__global__ void NOPK __launch_bounds__(512, 2) fused_kernel(const unsigned short* __restrict__ x, const unsigned short* __restrict__ wqkv,
                                                       const unsigned short* __restrict__ wo, const unsigned short* __restrict__ bx,
                                                       const unsigned short* __restrict__ fwr, const unsigned short* __restrict__ fwi,
                                                       unsigned short* __restrict__ out, char* ws) {
    extern __shared__ __attribute__((aligned(16))) char lds[];
    const char* pc0;
    asm volatile("s_getpc_b64 %0" : "=s"(pc0));
    const int tid = threadIdx.x, lane = tid & 63, wid = __builtin_amdgcn_readfirstlane(tid >> 6);
    const int bid = blockIdx.x, xcd = bid & 7;
    const int hl = (bid >> 3) & (HX - 1), head = (xcd / (HX / 2)) * HX + hl, ti = (xcd & (HX / 2 - 1)) * (32 / HX) + (bid >> 3) / HX;
    const int isv = hl / (HX / 2), pr = (head - hl) / 2 + (hl & (HX / 2 - 1));
    const unsigned lds0 = (unsigned)(uintptr_t)lds;
    const rsrc_t rs = __builtin_amdgcn_make_buffer_rsrc((void*)ws, (short)0, WS_END, 0x00020000);
    LAS unsigned* cnt = (LAS unsigned*)(lds + L_C);
    unsigned genv;
    asm volatile("global_load_dword %0, %1, %2 sc1" : "=v"(genv) : "v"(0), "s"(ws) : "memory");
    if (tid < 64) cnt[tid] = 0u;
    BAR();
    const int otile = xcd * 8 + (bid >> 5), cq = (bid >> 3) & 3;
    const int w = wid & 3;
    const int r = lane & 31, h = lane >> 5;
    unsigned tag;
    if (wid < 4) {
        __builtin_amdgcn_s_setprio(3);
        if (((bid >> 3) & 15) == 0 && tid < 232) {
            const unsigned c_ = *(const unsigned*)(pc0 + tid * 128);
            asm volatile("" :: "v"(c_));
        }
        const char* xsrc = (const char*)x + (size_t)(ti * 128 + w * 32 + (lane >> 3)) * 896;
        u32x4 xt[8];
#pragma unroll
        for (int i = 0; i < 8; ++i) {
            const char* g = xsrc + (i & 3) * (8 * 896) + (5 + (i >> 2)) * 128 + (((lane & 7) ^ ((4 * (i & 3) + (lane >> 4)) & 7)) * 16);
            xt[i] = *(const u32x4*)g;
        }
        const LAS char* xr = (const LAS char*)lds + L_XS + w * 20480 + r * 128;
        const int xk = (r >> 1) & 7;
        const int trow = 8 * h + ((lane & 15) >> 2), tcol = ((lane >> 4) & 1) * 32 + (lane & 3) * 8;
        const LAS char* wr = (const LAS char*)lds + L_WP + trow * 112 + tcol;
        f32x16 acc0 = f32x16{}, acc1 = f32x16{};
        bf16x8 xf[28], wf[8];
        u32x4 wq[7];
        const char* qs = (const char*)wqkv + ((56 * head) & ~15) + (size_t)(w * 112 + (lane >> 2)) * 2688 + (lane & 3) * 16;
        asm volatile("s_waitcnt vmcnt(8)\n\tv_readfirstlane_b32 %0, %1" : "=s"(tag) : "v"(genv) : "memory"); waitc(cnt + C_M + 1, 4u);
        tag += 1u;
#pragma unroll
        for (int kk = 0; kk < 4; ++kk) { wf[2 * kk] = trfrag(wr + kk * 16 * 112, 112); wf[2 * kk + 1] = trfrag(wr + kk * 16 * 112 + 56, 112); }
#pragma unroll
        for (int c = 0; c < 7; ++c) {
#define XRD(C) _Pragma("unroll") for (int kk = 0; kk < 4; ++kk) xf[4 * (C) + kk] = *(const LAS bf16x8*)(xr + ((C) % 5) * 4096 + (((2 * kk + h) ^ xk) * 16));
            if (c == 0) XRD(0)
            if (c == 1) waitc(cnt + C_M + 3, 4u); else if (c == 3) waitc(cnt + C_M + 6, 4u);
            if (c == 3) {
#pragma unroll
                for (int i = 0; i < 8; ++i) *(LAS u32x4*)(lds + L_XS + w * 20480 + i * 1024 + lane * 16) = xt[i];
                FENCE();
            }
            if (c < 6) XRD(c + 1)
            if (c >= 1 && c < 5) { wq[2 * c - 2] = *(const u32x4*)(qs + (2 * c - 2) * (16 * 2688)); if (c < 4) wq[2 * c - 1] = *(const u32x4*)(qs + (2 * c - 1) * (16 * 2688)); }
#define WNEXT if (c < 6) { wf[2 * kk] = trfrag(wr + ((c + 1) * 64 + kk * 16) * 112, 112); wf[2 * kk + 1] = trfrag(wr + ((c + 1) * 64 + kk * 16) * 112 + 56, 112); } SB();
            SB();
            {
#pragma unroll
                for (int kk = 0; kk < 4; ++kk) { acc0 = MFMA32(wf[2 * kk], xf[4 * c + kk], acc0); acc1 = MFMA32(wf[2 * kk + 1], xf[4 * c + kk], acc1); SB(); WNEXT }
            }
        }
        {
            const int h0 = 2 * pr, dst = WS_KV + (((h0 * 64 + ti * 4 + w) * 4 + (isv ? 2 : 0)) * 64 + lane) * 16;
#pragma unroll
            for (int j = 0; j < 2; ++j) {
                const f32x16 a = j ? acc1 : acc0;
                u32x4 f0, f1;
#pragma unroll
                for (int e = 0; e < 4; ++e) { f0[e] = cvtpk(a[2 * e], a[2 * e + 1]); f1[e] = cvtpk(a[8 + 2 * e], a[9 + 2 * e]); }
                if (h) { f1[2] = isv ? 0x00003f80u : 0x3f803f80u; f1[3] = isv ? 0u : 0x00003f80u; }
                if (isv) {
                    LAS char* tb = (LAS char*)lds + L_XS + w * 20480 + 8192 + j * 2048;
                    LAS u32x2* tw_ = (LAS u32x2*)(tb + r * 64 + h * 8);
                    tw_[0] = u32x2{f0[0], f0[1]}; tw_[2] = u32x2{f0[2], f0[3]}; tw_[4] = u32x2{f1[0], f1[1]}; tw_[6] = u32x2{f1[2], f1[3]};
                    WAITL();
                    const LAS char* tr_ = tb + (4 * h + ((lane & 15) >> 2)) * 64 + tcol;
                    f0 = __builtin_bit_cast(u32x4, trfrag(tr_, 128)); f1 = __builtin_bit_cast(u32x4, trfrag(tr_ + 1024, 128));
                    WAITL();
                }
                st128(rs, dst + j * 262144, f0); st128(rs, dst + j * 262144 + 1024, f1);
            }
        }
#pragma unroll
        for (int i = 0; i < 7; ++i) *(LAS u32x4*)(lds + L_XS + w * 20480 + 8192 + i * 1024 + lane * 16) = wq[i];
        arrive(cnt + C_WQ, lane); waitc(cnt + C_WQ, 4u);

        bf16x8 q0, q1;
        {
            const LAS char* qr = (const LAS char*)lds + L_XS + 8192 + trow * 64 + tcol + ((56 * head) & 15);
            f32x16 qa = f32x16{}, qb = f32x16{};
#pragma unroll
            for (int g = 0; g < 4; ++g) {
                if (g == 2) publish(rs, cnt + C_Q, F_KV + (pr * 32 + isv * 16 + ti) * 128, tag, lane, 1);
                bf16x8 wf[7];
#pragma unroll
                for (int kk = 0; kk < 7; ++kk) {
                    wf[kk] = trfrag(qr + g * 20480 + kk * 1024, 64);
                    const int ks = 7 * g + kk;
                    if (ks >= 16) xf[ks] = *(const LAS bf16x8*)(xr + ((ks >> 2) % 5) * 4096 + (((2 * (ks & 3) + h) ^ xk) * 16));
                }
                SB();
#pragma unroll
                for (int kk = 0; kk < 7; ++kk) { if (kk & 1) qb = MFMA32(wf[kk], xf[7 * g + kk], qb); else qa = MFMA32(wf[kk], xf[7 * g + kk], qa); }
            }
            qa = (qa + qb) * QSCALE;
            u32x4 f0, f1;
#pragma unroll
            for (int e = 0; e < 4; ++e) { f0[e] = cvtpk(qa[2 * e], qa[2 * e + 1]); f1[e] = cvtpk(qa[8 + 2 * e], qa[9 + 2 * e]); }
            if (h) { f1[2] = 0u; f1[3] = 0u; }
            q0 = BC8(f0); q1 = BC8(f1);
        }
        LAS bf16x8* qs_ = (LAS bf16x8*)(lds + L_WP) + lane;
        qs_[w * 128] = q0;
        if (w == 0) { poll32(rs, F_KV + ((head >> 1) * 32 + (lane & 31)) * 128, tag); arrive(cnt + C_PK, lane); }
        else waitc(cnt + C_PK, 1u);
        const int kvo = WS_KV + head * 262144 + lane * 16;
        bf16x8 k0, k1, v0, v1, v1p, n0, n1, n2, n3;
        f32x16 sa, sb, o, oo[4];
        {
            n0 = BC8(ld128(rs, kvo)); n1 = BC8(ld128(rs, kvo + 1024));
            const int t0 = kvo + w * 4096;
            k0 = BC8(ld128(rs, t0)); k1 = BC8(ld128(rs, t0 + 1024)); v0 = BC8(ld128(rs, t0 + 2048)); v1 = BC8(ld128(rs, t0 + 3072));
            sa = MFMA32(n0, q0, f32x16{}); sa = MFMA32(n1, q1, sa);
            n0 = BC8(ld128(rs, t0 + 16384)); n1 = BC8(ld128(rs, t0 + 17408)); n2 = BC8(ld128(rs, t0 + 18432)); n3 = BC8(ld128(rs, t0 + 19456));
            q1 = set_ref(q1, xhalf_max(max16(sa)), h);
            qs_[w * 128 + 64] = q1;
            arrive(cnt + C_QS, lane); waitc(cnt + C_QS, 4u);
        }
        bf16x8 qa[4], qb[4];
#pragma unroll
        for (int i = 0; i < 4; ++i) { qa[i] = qs_[i * 128]; qb[i] = qs_[i * 128 + 64]; oo[i] = f32x16{}; }
        __builtin_amdgcn_s_setprio(0);
        sa = MFMA32(k0, qa[0], f32x16{}); sa = MFMA32(k1, qb[0], sa);
        v1p = v1;
        u32x4 p0, p1 = u32x4{};
        float e0 = 0.f, e1 = 0.f, e2 = 0.f, e3 = 0.f;
#define E4(SA, i) e0 = EX(SA[i]); e1 = EX(SA[i + 1]); e2 = EX(SA[i + 2]); e3 = EX(SA[i + 3]); SB();
#define STEPQ(SA, SN, K0, K1, QN, OP, VP, OC, V0) \
        SN = MFMA32(K0, qa[QN], f32x16{}); SB(); p1[2] = cvtpk(e0, e1); p1[3] = cvtpk(e2, e3); SB(); \
        E4(SA, 0) \
        oo[OP] = MFMA32(VP, BC8(p1), oo[OP]); SB(); p0[0] = cvtpk(e0, e1); p0[1] = cvtpk(e2, e3); SB(); \
        E4(SA, 4) \
        SN = MFMA32(K1, qb[QN], SN); SB(); p0[2] = cvtpk(e0, e1); p0[3] = cvtpk(e2, e3); SB(); \
        E4(SA, 8) \
        oo[OC] = MFMA32(V0, BC8(p0), oo[OC]); SB(); p1[0] = cvtpk(e0, e1); p1[1] = cvtpk(e2, e3); SB(); \
        E4(SA, 12)
#define TILE(K0, K1, V0, V1, VP, N0, N1, OFF) \
        STEPQ(sa, sb, K0, K1, 1, 3, VP, 0, V0) \
        VP = BC8(ld128(rs, (OFF) - 16384 + 3072)); SB(); \
        STEPQ(sb, sa, K0, K1, 2, 0, V1, 1, V0) \
        STEPQ(sa, sb, K0, K1, 3, 1, V1, 2, V0) \
        K0 = BC8(ld128(rs, (OFF))); K1 = BC8(ld128(rs, (OFF) + 1024)); SB(); \
        STEPQ(sb, sa, N0, N1, 0, 2, V1, 3, V0) \
        V0 = BC8(ld128(rs, (OFF) + 2048)); SB();
        n3 = v1p;
#pragma unroll 1
        for (int i = 0; i < 8; ++i) {
            const int off = kvo + (((2 * i + 2) * 4 + w) & 63) * 4096;
            const int offb = i == 7 ? off + 64 * 4096 : off;
            STEPQ(sa, sb, k0, k1, 1, 3, n3, 0, v0)
            n3 = BC8(ld128(rs, offb - 16384 + 3072)); SB();
            STEPQ(sb, sa, k0, k1, 2, 0, v1, 1, v0)
            STEPQ(sa, sb, k0, k1, 3, 1, v1, 2, v0)
            k0 = BC8(ld128(rs, off)); k1 = BC8(ld128(rs, off + 1024)); SB();
            STEPQ(sb, sa, n0, n1, 0, 2, v1, 3, v0)
            v0 = BC8(ld128(rs, off + 2048)); SB();
            TILE(n0, n1, n2, n3, v1, k0, k1, off + 16384)
        }
        p1[2] = cvtpk(e0, e1); p1[3] = cvtpk(e2, e3);
        oo[3] = MFMA32(n3, BC8(p1), oo[3]);
        __builtin_amdgcn_s_setprio(3);
        {
            LAS f32x4* ox = (LAS f32x4*)(lds + 16384) + lane;
#pragma unroll
            for (int i = 0; i < 4; ++i) {
                if (i != w) {
#pragma unroll
                    for (int e = 0; e < 4; ++e) ox[((w * 4 + i) * 4 + e) * 64] = f32x4{oo[i][4 * e], oo[i][4 * e + 1], oo[i][4 * e + 2], oo[i][4 * e + 3]};
                } else o = oo[i];
            }
            arrive(cnt + C_OX, lane); waitc(cnt + C_OX, 4u);
#pragma unroll
            for (int i = 1; i < 4; ++i) {
#pragma unroll
                for (int e = 0; e < 4; ++e) { const f32x4 v = ox[((((w + i) & 3) * 4 + w) * 4 + e) * 64]; o[4 * e] += v[0]; o[4 * e + 1] += v[1]; o[4 * e + 2] += v[2]; o[4 * e + 3] += v[3]; }
            }
        }
        float l = hi_half(o[12]);
        float chk = 0.f;
#pragma unroll
        for (int e = 0; e < 16; ++e) chk += o[e] * 0.f;
        if (__builtin_amdgcn_ballot_w64(!(l > 0.25f && l < 1.0e18f && chk == 0.f)) != 0ull) {
            const int g = WS_KV + head * 262144 + lane * 16;
            q1 = set_ref(q1, 0.f, h);
            float m = -INFINITY;
#pragma unroll 1
            for (int t = 0; t < 64; ++t) {
                f32x16 s = MFMA32(BC8(ld128(rs, g + t * 4096)), q0, f32x16{});
                s = MFMA32(BC8(ld128(rs, g + t * 4096 + 1024)), q1, s);
                m = __builtin_fmaxf(m, max16(s));
            }
            q1 = set_ref(q1, xhalf_max(m), h);
            o = f32x16{};
#pragma unroll 1
            for (int t = 0; t < 64; ++t) {
                f32x16 s = MFMA32(BC8(ld128(rs, g + t * 4096)), q0, f32x16{});
                s = MFMA32(BC8(ld128(rs, g + t * 4096 + 1024)), q1, s);
#pragma unroll
                for (int e = 0; e < 4; ++e) { p0[e] = cvtpk(EX(s[2 * e]), EX(s[2 * e + 1])); p1[e] = cvtpk(EX(s[8 + 2 * e]), EX(s[9 + 2 * e])); }
                o = MFMA32(BC8(ld128(rs, g + t * 4096 + 2048)), BC8(p0), o);
                o = MFMA32(BC8(ld128(rs, g + t * 4096 + 3072)), BC8(p1), o);
            }
            l = hi_half(o[12]);
        }
        const float inv = 1.0f / l;
#pragma unroll
        for (int e = 0; e < 4; ++e) { p0[e] = cvtpk(o[2 * e] * inv, o[2 * e + 1] * inv); p1[e] = cvtpk(o[8 + 2 * e] * inv, o[9 + 2 * e] * inv); }
        {
            const int dst = WS_OF + ((((ti * 4 + w) * 16 + head) * 2) * 64 + lane) * 16;
            waitc(cnt + C_R0, 4u);
            st128(rs, dst, p0); st128(rs, dst + 1024, p1);
        }
        publish(rs, cnt + C_O, F_O + (ti * 16 + head) * 128, tag, lane, 1);

    } else {
        __builtin_amdgcn_s_setprio(2);
        const int b = bid, t = tid - 256;
        unsigned wv[8];
        LAS cf* fb = (LAS cf*)(lds + L_F);
        LAS cf* f = fb + w * 7 * RSTR;
        const int l2 = lane < 56 ? lane : 55;
        const int n2 = 2 * t, pos = (n2 >> 6) * RSTR + (n2 & 63);
        const float rr = 0.70710678118654752f;
        const int s2 = l2 >> 3, r0 = l2 & 7, m1 = l2 / 7, s3 = l2 - 7 * m1;
        const int j = 8 * w + (lane >> 3), j0 = (lane >> 3) & 3, d0 = 2 * w + (lane >> 5);
        cf a[8], t448[7], t64[8], t256[8], t32[8], tb1, tb2, tb3, tb4;
        {
            unsigned xv[8];
            const int tc = t < 224 ? t : 223;
#pragma unroll
            for (int i = 0; i < 8; ++i) xv[i] = *(const unsigned*)(bx + (size_t)(256 * i + b) * 448 + 2 * tc);
            {
                const char* wsrc = (const char*)wqkv + 896 * (1 + isv) + 112 * pr;
                const char* xsrc = (const char*)x + (size_t)(ti * 128 + w * 32 + (lane >> 3)) * 896;
                const int pa = 2 * w, pb = w < 3 ? 2 * w + 1 : 6;
                const int ca = pa * 64 + lane, cb_ = pb * 64 + lane;
                const int oa = (ca / 7) * 2688 + (ca % 7) * 16, ob = (cb_ / 7) * 2688 + (cb_ % 7) * 16;
                const int x0 = ((lane & 7) ^ (lane >> 4)) * 16, x1 = ((lane & 7) ^ (4 + (lane >> 4))) * 16;
                auto ldc = [&](int c) __attribute__((always_inline)) {
                    glds16(wsrc + c * (64 * 2688) + oa, lds0 + L_WP + (c * 7 + pa) * 1024);
                    glds16(wsrc + c * (64 * 2688) + ob, lds0 + L_WP + (c * 7 + pb) * 1024);
                    if (c < 5) {
#pragma unroll
                        for (int i = 0; i < 4; ++i) glds16(xsrc + i * (8 * 896) + c * 128 + ((i & 1) ? x1 : x0), lds0 + L_XS + w * 20480 + c * 4096 + i * 1024);
                    }
                };
                ldc(0); ldc(1); ldc(2); ldc(3); ldc(5); ldc(6);
                WAITV(16); arrive(cnt + C_M + 1, lane); ldc(4);
                WAITV(10); arrive(cnt + C_M + 3, lane);
                WAITV(0); arrive(cnt + C_M + 6, lane);
            }
            const int item = bid * 112 + (t < 112 ? t : 111), l = item & 63, fr = item >> 6, hd = fr & 15, cb = fr >> 4;
            const int g = l >> 4, dd = 16 * (g >> 1) + 4 * (g & 1);
            const unsigned short* src = wo + (size_t)(hd * 28) * 448 + cb * 16 + (l & 15);
            if (w < 2) {
#pragma unroll
                for (int e = 0; e < 8; ++e) { const int d = dd + 8 * (e >> 2) + (e & 3); wv[e] = src[(d < 28 ? d : 0) * 448]; }
            }
#pragma unroll
            for (int i = 1; i < 8; ++i) { if (i < 7) t448[i] = tw(lane * i, 1.f / 448); t64[i] = tw(r0 * i, 1.f / 64); t256[i] = tw(j * i, 1.f / 256); t32[i] = tw(j0 * i, 1.f / 32); }
            tb1 = tw(b, 1.f / 2048); tb2 = tw(2 * b, 1.f / 2048); tb3 = tw(3 * b, 1.f / 2048); tb4 = tw(b, 1.f / 512);
#pragma unroll
            for (int i = 1; i < 8; ++i) { if (i < 7) asm volatile("" : "+v"(t448[i])); asm volatile("" : "+v"(t64[i]), "+v"(t256[i]), "+v"(t32[i])); }
            asm volatile("" : "+v"(tb1), "+v"(tb2), "+v"(tb3), "+v"(tb4));
            if (t < 224) {
                cf v[8];
#pragma unroll
                for (int i = 0; i < 8; ++i) v[i] = cf{bflo(xv[i]), bfhi(xv[i])};
                cf p0 = v[0] + v[4], m0 = v[0] - v[4], p1 = v[1] + v[5], n1 = v[1] - v[5];
                cf p2 = v[2] + v[6], m2 = v[2] - v[6], p3 = v[3] + v[7], m3 = v[3] - v[7];
                cf e0 = p0 + p2, e1 = p1 + p3;
                cf z0 = e0 + e1, z4 = e0 - e1;
                cf z2r = p0 - p2, z2i = p3 - p1;
                cf qa = (n1 - m3) * rr, qb = (n1 + m3) * rr;
                cf z1r = m0 + qa, z1i = -m2 - qb, z3r = m0 - qa, z3i = m2 - qb;
                const cf w1 = tb1, w2 = tb2, w3 = tb3;
                cf a0, a1;
                *(LAS f32x4*)(fb + pos) = f32x4{z0.x, z4.x, z0.y, z4.y};
                a0 = cmul<-1>(cf{z1r.x, z1i.x}, w1); a1 = cmul<-1>(cf{z1r.y, z1i.y}, w1);
                *(LAS f32x4*)(fb + 7 * RSTR + pos) = f32x4{a0.x, a0.y, a1.x, a1.y};
                a0 = cmul<-1>(cf{z2r.x, z2i.x}, w2); a1 = cmul<-1>(cf{z2r.y, z2i.y}, w2);
                *(LAS f32x4*)(fb + 14 * RSTR + pos) = f32x4{a0.x, a0.y, a1.x, a1.y};
                a0 = cmul<-1>(cf{z3r.x, z3i.x}, w3); a1 = cmul<-1>(cf{z3r.y, z3i.y}, w3);
                *(LAS f32x4*)(fb + 21 * RSTR + pos) = f32x4{a0.x, a0.y, a1.x, a1.y};
            }
            arrive(cnt + C_B0, lane);
            if (w >= 2 && lane < 40) {
                int e = (8 * (8 * (bid >> 3) + lane / 5) + xcd) * 225 + 7 * xcd + (lane % 5) * 64;
                e = e < 2048 * 225 ? e : 2048 * 225 - 1;
                wv[0] = (w == 2 ? fwr : fwi)[e];
            }
            waitc(cnt + C_B0, 4u);
        }
#pragma unroll
        for (int p = 0; p < 7; ++p) a[p] = f[p * RSTR + lane];
        dft7<-1>(a);
#pragma unroll
        for (int s = 1; s < 7; ++s) a[s] = cmul<-1>(a[s], t448[s]);
#pragma unroll
        for (int s = 0; s < 7; ++s) f[s * RSTR + lane] = a[s];
        WAITL();
#pragma unroll
        for (int i = 0; i < 8; ++i) a[i] = f[s2 * RSTR + 8 * i + r0];
        dft8<-1>(a);

#pragma unroll
        for (int i = 1; i < 8; ++i) a[i] = cmul<-1>(a[i], t64[i]);
        if (lane < 56) {
#pragma unroll
            for (int i = 0; i < 8; ++i) f[s2 * RSTR + 8 * i + r0] = a[i];
        }
        WAITL();
#pragma unroll
        for (int i = 0; i < 8; ++i) a[i] = f[s3 * RSTR + 8 * m1 + i];
        dft8<-1>(a);
        {
            LAS cf* y = (LAS cf*)(lds + L_YA);
            if (w != 0) {
                if (lane < 56) {
#pragma unroll
                    for (int i = 0; i < 4; ++i) y[w * 225 + lane + 56 * i] = a[i];
#pragma unroll
                    for (int i = 4; i < 8; ++i) y[(8 - w) * 225 + 448 - lane - 56 * i] = cconj(a[i]);
                    if (lane == 0) { y[w * 225 + 224] = a[4]; y[(8 - w) * 225] = cconj(a[0]); }
                }
            } else {
                WAITL();
                if (lane < 56) {
#pragma unroll
                    for (int i = 0; i < 8; ++i) f[lane + 56 * i] = a[i];
                }
                if (lane == 0) f[448] = a[0];
                WAITL();
                const cf w4 = cf{tb4.y, -tb4.x} * 0.5f;
#pragma unroll
                for (int i = 0; i < 4; ++i) {
                    const int k = lane + 64 * i;
                    if (k <= 224) {
                        const cf u = f[k], v = cconj(f[448 - k]);
                        y[k] = (u + v) * 0.5f;
                        y[4 * 225 + k] = cmul<-1>(u - v, w4);
                    }
                }
            }
            arrive(cnt + C_B1, lane); waitc(cnt + C_B1, 4u);
#pragma unroll
            for (int i = 0; i < 4; ++i) { const int e = t + 256 * i; if (e < 900) st128(rs, WS_Y1 + b * (NQ * 8) + e * 16, ((LAS u32x4*)y)[e]); }
            const bool pad = ((bid * 112 + t) & 48) == 48;
            if (t < 112) st128(rs, WS_WO + (bid * 112 + t) * 16, u32x4{wv[0] | (wv[1] << 16), wv[2] | (wv[3] << 16), pad ? 0u : wv[4] | (wv[5] << 16), pad ? 0u : wv[6] | (wv[7] << 16)});
        }
        asm volatile("s_waitcnt vmcnt(0)\n\tv_readfirstlane_b32 %0, %1" : "=s"(tag) : "v"(genv) : "memory");
        asm volatile("" :: "v"(wv[0]));
        tag += 1u;
        publish(rs, cnt + C_B3, F_Y1 + bid * 4, tag, lane, 8);
        waitc(cnt + C_PK, 1u);
        {
            const int grp = (bid & 7) * 29 + (bid >> 3);
            const bool act = (bid >> 3) < 29 && grp < 225;
            const int col = lane & 7, q = (act ? grp : 0) * 8 + col, c = q / 225, k2 = q - c * 225, cs = c > 4 ? c - 8 : c;
            {
                const int dst = WS_OF + ((((ti * 4 + w) * 16 + head) * 2) * 64 + lane) * 16;
                st128(rs, dst, u32x4{}); st128(rs, dst + 1024, u32x4{});
            }
            unsigned fr_[8], fi_[8];
#pragma unroll
            for (int i = 0; i < 8; ++i) {
                const int d = d0 + 8 * (j0 + 4 * (i >> 2)) + 64 * (i & 3), k1 = (cs + 8 * d) & 2047;
                fr_[i] = fwr[k1 * 225 + k2]; fi_[i] = fwi[k1 * 225 + k2];
                __builtin_amdgcn_s_sleep(3); FENCE();
            }
            if (w == 0) {
                poll128(rs, F_Y1 + xcd * FREP + lane * 16, tag);
                arrive(cnt + C_B2, lane);
            } else waitc(cnt + C_B2, 1u);
            if (t < 112) { wv[0] = ld32(rs, WS_WO + ((bid >> 3) * 112 + t) * 128); asm volatile("" :: "v"(wv[0])); }
            LAS cf* mb = fb;
#pragma unroll
            for (int i = 0; i < 8; ++i) a[i] = ld64(rs, WS_Y1 + ((32 * i + j) * NQ + q) * 8);
            if (act) {
                dft8<-1>(a);
#pragma unroll
                for (int i = 1; i < 8; ++i) a[i] = cmul<-1>(a[i], t256[i]);
#pragma unroll
                for (int i = 0; i < 8; ++i) mb[(i * 32 + j) * 8 + col] = a[i];
                arrive(cnt + C_B4, lane); waitc(cnt + C_B4, 4u);
#pragma unroll
                for (int i = 0; i < 8; ++i) a[i] = mb[(d0 * 32 + 4 * i + j0) * 8 + col];
                dft8<-1>(a);
#pragma unroll
                for (int i = 1; i < 8; ++i) a[i] = cmul<-1>(a[i], t32[i]);
#pragma unroll
                for (int i = 0; i < 8; ++i) mb[(d0 * 32 + 4 * i + j0) * 8 + col] = a[i];
                WAITL();
#pragma unroll
                for (int i = 0; i < 8; ++i) a[i] = mb[(d0 * 32 + 4 * (j0 + 4 * (i >> 2)) + (i & 3)) * 8 + col];
                dft4<-1>(a[0], a[1], a[2], a[3]); dft4<-1>(a[4], a[5], a[6], a[7]);
#pragma unroll
                for (int i = 0; i < 8; ++i) a[i] = cmul<1>(a[i], cf{bflo(fi_[i]), bflo(fr_[i])});
                dft4<1>(a[0], a[1], a[2], a[3]); dft4<1>(a[4], a[5], a[6], a[7]);
#pragma unroll
                for (int i = 0; i < 8; ++i) mb[(d0 * 32 + 4 * (j0 + 4 * (i >> 2)) + (i & 3)) * 8 + col] = a[i];
                WAITL();
#pragma unroll
                for (int i = 0; i < 8; ++i) a[i] = mb[(d0 * 32 + 4 * i + j0) * 8 + col];
#pragma unroll
                for (int i = 1; i < 8; ++i) a[i] = cmul<1>(a[i], t32[i]);
                dft8<1>(a);
#pragma unroll
                for (int i = 0; i < 8; ++i) mb[(d0 * 32 + 4 * i + j0) * 8 + col] = a[i];
                arrive(cnt + C_B5, lane); waitc(cnt + C_B5, 4u);
#pragma unroll
                for (int i = 0; i < 8; ++i) a[i] = mb[(i * 32 + j) * 8 + col];
#pragma unroll
                for (int i = 1; i < 8; ++i) a[i] = cmul<1>(a[i], t256[i]);
                dft8<1>(a);
#pragma unroll
                for (int i = 0; i < 8; ++i) st64(rs, WS_Y2 + ((32 * i + j) * NQ + q) * 8, a[i]);
            }
        }
        publish(rs, cnt + C_R0, F_Y2 + bid * 4, tag, lane, 8);
        bf16x8 wa[16], wb[16];
        const int cb0 = cq * 7 + w, cb1 = cq * 7 + (w < 3 ? w + 4 : 6);
#pragma unroll
        for (int i = 0; i < 16; ++i) { wa[i] = BC8(ld128(rs, WS_WO + ((cb0 * 16 + i) * 64 + lane) * 16)); wb[i] = BC8(ld128(rs, WS_WO + ((cb1 * 16 + i) * 64 + lane) * 16)); __builtin_amdgcn_s_sleep(5); FENCE(); }
        waitc(cnt + C_R0, 4u);
        if (w == 0) {
            poll128(rs, F_Y2 + xcd * FREP + lane * 16, tag);
            arrive(cnt + C_PY, lane);
            if (bid == 0 && lane == 0) st32(rs, F_GEN, tag);
        } else waitc(cnt + C_PY, 1u);
        {
            const int y = WS_Y2 + b * (NQ * 8);
            cf z[8];
            const int ya = y + (w * 225) * 8, yb = y + ((w ? 8 - w : 0) * 225) * 8;
#pragma unroll
            for (int i = 0; i < 8; ++i) a[i] = ld64(rs, i < 4 ? ya + (l2 + 56 * i) * 8 : yb + (448 - l2 - 56 * i) * 8);
            if (w) { z[0] = ld64(rs, yb); z[4] = ld64(rs, ya + 224 * 8); }
            else {
#pragma unroll
                for (int i = 0; i < 8; ++i) z[i] = ld64(rs, y + (900 + (i < 4 ? l2 + 56 * i : 448 - l2 - 56 * i)) * 8);
            }
            waitc(cnt + C_OX, 1u);
#pragma unroll
            for (int i = 4; i < 8; ++i) a[i] = cconj(a[i]);
            if (w) { if (lane == 0) { a[0] = (a[0] + cconj(z[0])) * 0.5f; a[4] = (a[4] + z[4]) * 0.5f; } }
            else {
                const cf w4 = tb4;
#pragma unroll
                for (int i = 0; i < 8; ++i) {
                    cf z4 = cmul<1>(z[i], w4);
                    if (i >= 4) z4 = cconj(z4);
                    if ((i & 3) == 0 && lane == 0) { a[i].y = 0.f; z4.y = 0.f; }
                    a[i] = addrot<1>(a[i], z4);
                }
            }
        }
        dft8<1>(a);
        if (lane < 56) {
#pragma unroll
            for (int i = 0; i < 8; ++i) f[s3 * RSTR + 8 * m1 + i] = a[i];
        }
        WAITL();
#pragma unroll
        for (int i = 0; i < 8; ++i) a[i] = f[s2 * RSTR + 8 * i + r0];
#pragma unroll
        for (int i = 1; i < 8; ++i) a[i] = cmul<1>(a[i], t64[i]);
        dft8<1>(a);
        if (lane < 56) {
#pragma unroll
            for (int i = 0; i < 8; ++i) f[s2 * RSTR + 8 * i + r0] = a[i];
        }
        WAITL();
#pragma unroll
        for (int s = 0; s < 7; ++s) a[s] = f[s * RSTR + lane];
#pragma unroll
        for (int s = 1; s < 7; ++s) a[s] = cmul<1>(a[s], t448[s]);
        dft7<1>(a);
#pragma unroll
        for (int p = 0; p < 7; ++p) f[p * RSTR + lane] = a[p];
        arrive(cnt + C_B6, lane); waitc(cnt + C_B6, 4u);
        if (t < 224) {
            const float sc = 1.0f / (2048.0f * 448.0f), sc2 = 2.f * sc;
            const f32x4 u0 = *(LAS f32x4*)(fb + pos), u1 = *(LAS f32x4*)(fb + 7 * RSTR + pos), u2 = *(LAS f32x4*)(fb + 14 * RSTR + pos), u3 = *(LAS f32x4*)(fb + 21 * RSTR + pos);
            const cf w1 = tb1 * sc2, w2 = tb2 * sc2, w3 = tb3 * sc2;
            const cf z0 = cf{u0[0], u0[2]} * sc, z4 = cf{u0[1], u0[3]} * sc;
            cf a0 = cmul<1>(cf{u1[0], u1[1]}, w1), a1 = cmul<1>(cf{u1[2], u1[3]}, w1);
            const cf z1r = cf{a0.x, a1.x}, z1i = cf{a0.y, a1.y};
            a0 = cmul<1>(cf{u2[0], u2[1]}, w2); a1 = cmul<1>(cf{u2[2], u2[3]}, w2);
            const cf z2r = cf{a0.x, a1.x}, z2i = cf{a0.y, a1.y};
            a0 = cmul<1>(cf{u3[0], u3[1]}, w3); a1 = cmul<1>(cf{u3[2], u3[3]}, w3);
            const cf z3r = cf{a0.x, a1.x}, z3i = cf{a0.y, a1.y};
            const cf e0 = z0 + z4, e1 = z0 - z4;
            const cf p0 = e0 + z2r, p2 = e0 - z2r, p1 = e1 - z2i, p3 = e1 + z2i;
            const cf m0 = z1r + z3r, m2 = z3i - z1i;
            const cf qa = (z1r - z3r) * rr, qb = (z1i + z3i) * rr;
            const cf mm1 = qa - qb, m3 = -qa - qb;
            const cf yv[8] = {p0 + m0, p1 + mm1, p2 + m2, p3 + m3, p0 - m0, p1 - mm1, p2 - m2, p3 - m3};
#pragma unroll
            for (int i = 0; i < 8; ++i) *(unsigned*)(out + (size_t)(256 * i + b) * 896 + 448 + n2) = cvtpk(yv[i].x, yv[i].y);
        }
        waitc(cnt + C_OX, 4u);
        __builtin_amdgcn_s_setprio(3);
        if (w == 0) { poll32(rs, F_O + ((otile >> 2) * 16 + (lane & 15)) * 128, tag); arrive(cnt + C_PO, lane); }
        else waitc(cnt + C_PO, 1u);
        u32x4 ov[8];
#pragma unroll
        for (int i = 0; i < 8; ++i) ov[i] = ld128(rs, WS_OF + (otile * 2048 + (i * 4 + w) * 64 + lane) * 16);
#pragma unroll
        for (int i = 0; i < 8; ++i) ((LAS u32x4*)(lds + 98304))[(i * 4 + w) * 64 + lane] = ov[i];
        arrive(cnt + C_OS, lane); waitc(cnt + C_OS, 4u);
        {
            const int g = lane >> 4;
            const LAS bf16x8* op = (const LAS bf16x8*)(lds + 98304) + (g >> 1) * 64 + (g & 1) * 32 + (lane & 15);
            f32x4 c0 = f32x4{}, c1 = f32x4{}, c2 = f32x4{}, c3 = f32x4{};
#pragma unroll
            for (int i = 0; i < 16; ++i) {
                const bf16x8 a0 = op[i * 128], a1 = op[i * 128 + 16];
                c0 = MFMA16(wa[i], a0, c0); c1 = MFMA16(wa[i], a1, c1); c2 = MFMA16(wb[i], a0, c2); c3 = MFMA16(wb[i], a1, c3);
            }
            unsigned short* dst = out + (size_t)(otile * 32 + (lane & 15)) * 896 + g * 4;
            *(u32x2*)(dst + cb0 * 16) = u32x2{cvtpk(c0[0], c0[1]), cvtpk(c0[2], c0[3])};
            *(u32x2*)(dst + cb0 * 16 + 16 * 896) = u32x2{cvtpk(c1[0], c1[1]), cvtpk(c1[2], c1[3])};
            if (w < 3) {
                *(u32x2*)(dst + cb1 * 16) = u32x2{cvtpk(c2[0], c2[1]), cvtpk(c2[2], c2[3])};
                *(u32x2*)(dst + cb1 * 16 + 16 * 896) = u32x2{cvtpk(c3[0], c3[1]), cvtpk(c3[2], c3[3])};
            }
        }
    }
}

extern "C" void kernel_launch(void* const* d_in, const int* in_sizes, int n_in, void* d_out, int out_size, void* d_ws,
                              size_t ws_size, hipStream_t stream) {
    (void)hipFuncSetAttribute((const void*)fused_kernel, hipFuncAttributeMaxDynamicSharedMemorySize, L_END);
    fused_kernel<<<256, 512, L_END, stream>>>((const unsigned short*)d_in[0], (const unsigned short*)d_in[1], (const unsigned short*)d_in[2],
                                              (const unsigned short*)d_in[3], (const unsigned short*)d_in[4], (const unsigned short*)d_in[5],
                                              (unsigned short*)d_out, (char*)d_ws);
}
```

```cpp
#include <hip/hip_runtime.h>
#include <hip/hip_bf16.h>
#include <stdint.h>

typedef __attribute__((ext_vector_type(8))) short bf16x8;
typedef __attribute__((ext_vector_type(4))) short s16x4;
typedef __attribute__((ext_vector_type(16))) float f32x16;
typedef __attribute__((ext_vector_type(4))) float f32x4;
typedef __attribute__((ext_vector_type(4))) unsigned u32x4;
typedef __attribute__((ext_vector_type(2))) unsigned u32x2;
typedef __attribute__((ext_vector_type(2))) float cf;
#define LAS __attribute__((address_space(3)))
#if defined(__HIP_DEVICE_COMPILE__)
#define NOPK __attribute__((target("no-packed-fp32-ops")))
#else
#define NOPK
#endif
#define DEVI __device__ __forceinline__
#define MFMA32(a, b, c) __builtin_amdgcn_mfma_f32_32x32x16_bf16(a, b, c, 0, 0, 0)
#define MFMA16(a, b, c) __builtin_amdgcn_mfma_f32_16x16x32_bf16(a, b, c, 0, 0, 0)

constexpr int NQ = 1800;
constexpr int F_GEN = 0, F_KV = 256, F_O = F_KV + 256 * 128, F_Y1 = F_O + 256 * 128, FREP = 1024 + 256, F_Y2 = F_Y1 + 8 * FREP;
constexpr int WS_KV = 131072;
constexpr int WS_WO = WS_KV + 16 * 64 * 4 * 1024;
constexpr int WS_OF = WS_WO + 28 * 16 * 1024;
constexpr int WS_Y1 = WS_OF + 64 * 16 * 2 * 1024;
constexpr int WS_Y2 = WS_Y1;
constexpr int WS_END = WS_Y1 + 256 * NQ * 8;
constexpr float QSCALE = 0.18898223650461362f * 1.4426950408889634f;

typedef __attribute__((ext_vector_type(2))) __bf16 bf16x2_t;
DEVI unsigned cvtpk(float lo, float hi) { return __builtin_bit_cast(unsigned, __builtin_convertvector(cf{lo, hi}, bf16x2_t)); }
DEVI float bflo(unsigned u) { return __uint_as_float(u << 16); }
DEVI float bfhi(unsigned u) { return __uint_as_float(u & 0xffff0000u); }
DEVI void glds16(const void* g, unsigned lds_base) {
    unsigned sv;
    asm volatile("s_mov_b32 %0, m0\n\ts_mov_b32 m0, %2\n\ts_nop 0\n\tglobal_load_lds_dwordx4 %1, off\n\ts_mov_b32 m0, %0"
                 : "=&s"(sv) : "v"(g), "s"(__builtin_amdgcn_readfirstlane(lds_base)) : "memory");
}
#define WAITV(N) asm volatile("s_waitcnt vmcnt(" #N ")" ::: "memory")
#define WAITL() asm volatile("s_waitcnt lgkmcnt(0)" ::: "memory")
#define BAR() asm volatile("s_waitcnt lgkmcnt(0)\n\ts_barrier" ::: "memory")

template <int DIR> DEVI cf cmul(cf a, cf w) {
    return DIR > 0 ? cf{__builtin_fmaf(a.x, w.y, -a.y * w.x), __builtin_fmaf(a.y, w.y, a.x * w.x)}
                   : cf{__builtin_fmaf(a.x, w.y, a.y * w.x), __builtin_fmaf(a.y, w.y, -a.x * w.x)};
}
template <int DIR> DEVI cf addrot(cf t, cf x) { return DIR > 0 ? cf{t.x - x.y, t.y + x.x} : cf{t.x + x.y, t.y - x.x}; }
DEVI cf cconj(cf a) { return cf{a.x, -a.y}; }
DEVI cf tw(int num, float inv_den) {
    const float x = (float)num * inv_den;
    return cf{__builtin_amdgcn_sinf(x), __builtin_amdgcn_cosf(x)};
}
template <int DIR> DEVI void dft4(cf& a0, cf& a1, cf& a2, cf& a3) {
    cf t0 = a0 + a2, t1 = a0 - a2, t2 = a1 + a3, t3 = a1 - a3;
    a0 = t0 + t2; a2 = t0 - t2; a1 = addrot<DIR>(t1, t3); a3 = addrot<-DIR>(t1, t3);
}
template <int DIR> DEVI void dft8(cf* a) {
    cf e0 = a[0], e1 = a[2], e2 = a[4], e3 = a[6], o0 = a[1], o1 = a[3], o2 = a[5], o3 = a[7];
    dft4<DIR>(e0, e1, e2, e3); dft4<DIR>(o0, o1, o2, o3);
    const float r = 0.70710678118654752f;
    o1 = addrot<DIR>(o1, o1); o3 = addrot<-DIR>(o3, o3);
    a[0] = e0 + o0; a[4] = e0 - o0; a[1] = e1 + o1 * r; a[5] = e1 - o1 * r;
    a[2] = addrot<DIR>(e2, o2); a[6] = addrot<-DIR>(e2, o2); a[3] = e3 - o3 * r; a[7] = e3 + o3 * r;
}
template <int DIR> DEVI void dft7(cf* a) {
    const float c1 = 0.62348980185873353f, c2 = -0.22252093395631440f, c3 = -0.90096886790241913f;
    const float s1 = 0.78183148246802981f, s2 = 0.97492791218182361f, s3 = 0.43388373911755812f;
    cf t1 = a[1] + a[6], t2 = a[2] + a[5], t3 = a[3] + a[4], u1 = a[1] - a[6], u2 = a[2] - a[5], u3 = a[3] - a[4];
    cf A1 = a[0] + t1 * c1 + t2 * c2 + t3 * c3, A2 = a[0] + t1 * c2 + t2 * c3 + t3 * c1, A3 = a[0] + t1 * c3 + t2 * c1 + t3 * c2;
    cf B1 = u1 * s1 + u2 * s2 + u3 * s3, B2 = u1 * s2 - u2 * s3 - u3 * s1, B3 = u1 * s3 - u2 * s1 + u3 * s2;
    a[0] = a[0] + t1 + t2 + t3;
    a[1] = addrot<DIR>(A1, B1); a[6] = addrot<-DIR>(A1, B1); a[2] = addrot<DIR>(A2, B2); a[5] = addrot<-DIR>(A2, B2);
    a[3] = addrot<DIR>(A3, B3); a[4] = addrot<-DIR>(A3, B3);
}
constexpr int RSTR = 72;
constexpr int RFB = 7 * RSTR * 8;

typedef __amdgpu_buffer_rsrc_t rsrc_t;
DEVI void st128(rsrc_t r, int off, u32x4 v) { __builtin_amdgcn_raw_buffer_store_b128(v, r, off, 0, 16); }
DEVI void st64(rsrc_t r, int off, cf v) { __builtin_amdgcn_raw_buffer_store_b64(__builtin_bit_cast(u32x2, v), r, off, 0, 16); }
DEVI void st32(rsrc_t r, int off, unsigned v) { __builtin_amdgcn_raw_buffer_store_b32(v, r, off, 0, 16); }
DEVI u32x4 ld128(rsrc_t r, int off) { return __builtin_amdgcn_raw_buffer_load_b128(r, off, 0, 16); }
DEVI cf ld64(rsrc_t r, int off) { return __builtin_bit_cast(cf, __builtin_amdgcn_raw_buffer_load_b64(r, off, 0, 16)); }
DEVI unsigned ld32(rsrc_t r, int off) { return __builtin_amdgcn_raw_buffer_load_b32(r, off, 0, 16); }
#define FENCE() asm volatile("" ::: "memory")
constexpr int SPIN_CAP = 1 << 18;
DEVI void arrive(LAS unsigned* c, int) {
    asm volatile("s_waitcnt lgkmcnt(0)\n\ts_mov_b64 exec, 1\n\tds_add_u32 %0, %1\n\ts_mov_b64 exec, -1" :: "v"(c), "v"(1u) : "memory");
}
DEVI unsigned arrive_rtn(LAS unsigned* c) {
    unsigned v, r;
    asm volatile("s_waitcnt lgkmcnt(0)\n\ts_mov_b64 exec, 1\n\tds_add_rtn_u32 %0, %2, %3\n\ts_waitcnt lgkmcnt(0)\n\tv_readfirstlane_b32 %1, %0\n\ts_mov_b64 exec, -1"
                 : "=&v"(v), "=s"(r) : "v"(c), "v"(1u) : "memory");
    return r;
}
DEVI void waitc(LAS unsigned* c, unsigned n) {
    unsigned v, sv, it;
    asm volatile("s_mov_b32 %2, 0x200000\n"
                 "1:\n\tds_read_b32 %0, %3\n\ts_waitcnt lgkmcnt(0)\n\tv_readfirstlane_b32 %1, %0\n\ts_cmp_ge_u32 %1, %4\n\ts_cbranch_scc1 2f\n\t"
                 "s_sleep 3\n\ts_sub_u32 %2, %2, 1\n\ts_cbranch_scc0 1b\n2:"
                 : "=&v"(v), "=&s"(sv), "=&s"(it) : "v"(c), "s"(n) : "memory", "scc");
}
DEVI void publish(rsrc_t r, LAS unsigned* c, int flag_off, unsigned tag, int lane, int nrep) {
    WAITV(0);
    if (arrive_rtn(c) == 3u && lane < nrep) st32(r, flag_off + lane * FREP, tag);
}
DEVI void poll32(rsrc_t r, int off, unsigned tag) {
#pragma unroll 1
    for (int i = 0; i < SPIN_CAP; ++i) { if (__builtin_amdgcn_ballot_w64(ld32(r, off) != tag) == 0ull) break; __builtin_amdgcn_s_sleep(2); FENCE(); }
    FENCE();
}
DEVI void poll128(rsrc_t r, int off, unsigned tag) {
#pragma unroll 1
    for (int i = 0; i < SPIN_CAP; ++i) {
        const u32x4 v = ld128(r, off);
        if (__builtin_amdgcn_ballot_w64(!(v[0] == tag && v[1] == tag && v[2] == tag && v[3] == tag)) == 0ull) break;
        __builtin_amdgcn_s_sleep(10); FENCE();
    }
    FENCE();
}

DEVI float xhalf_max(float m) {
    auto rr = __builtin_amdgcn_permlane32_swap(__float_as_uint(m), __float_as_uint(m), false, false);
    return __builtin_fmaxf(__uint_as_float(rr[0]), __uint_as_float(rr[1]));
}
DEVI float hi_half(float m) {
    auto rr = __builtin_amdgcn_permlane32_swap(__float_as_uint(m), __float_as_uint(m), false, false);
    return __uint_as_float(rr[1]);
}
DEVI float max16(const f32x16& s) {
    float m = __builtin_fmaxf(s[0], s[1]);
#pragma unroll
    for (int i = 2; i < 16; ++i) m = __builtin_fmaxf(m, s[i]);
    return m;
}
DEVI bf16x8 set_ref(bf16x8 q1, float m, int h) {
    const float nm = -m;
    const unsigned b0 = __float_as_uint(nm) & 0xffff0000u; const float r1 = nm - __uint_as_float(b0);
    const unsigned b1 = __float_as_uint(r1) & 0xffff0000u; const float r2 = r1 - __uint_as_float(b1);
    const unsigned b2 = __float_as_uint(r2) & 0xffff0000u;
    u32x4 u = __builtin_bit_cast(u32x4, q1);
    if (h) { u[2] = (b0 >> 16) | b1; u[3] = b2 >> 16; }
    return __builtin_bit_cast(bf16x8, u);
}
DEVI bf16x8 trfrag(const LAS char* p, int rowstride) {
    s16x4 lo = __builtin_amdgcn_ds_read_tr16_b64_v4i16((LAS s16x4*)p);
    s16x4 hi = __builtin_amdgcn_ds_read_tr16_b64_v4i16((LAS s16x4*)(p + 4 * rowstride));
    return __builtin_shufflevector(lo, hi, 0, 1, 2, 3, 4, 5, 6, 7);
}
#define SB() __builtin_amdgcn_sched_barrier(0)
#define EX(x) __builtin_amdgcn_exp2f(x)
#define BC8(x) __builtin_bit_cast(bf16x8, x)

#define HX 2
constexpr int L_WP = 0, L_XS = 50240, L_F = L_XS + 4 * 20480, L_YA = L_F + 16384, L_C = L_YA + 14400, L_END = L_C + 256;
enum { C_Q = 0, C_PK, C_PO, C_PY, C_WQ, C_R0, C_O, C_OS, C_QS, C_OX, C_B0, C_B1, C_B2, C_B3, C_B4, C_B5, C_B6, C_M = 40 };

__global__ void NOPK __launch_bounds__(512, 2) fused_kernel(const unsigned short* __restrict__ x, const unsigned short* __restrict__ wqkv,
                                                       const unsigned short* __restrict__ wo, const unsigned short* __restrict__ bx,
                                                       const unsigned short* __restrict__ fwr, const unsigned short* __restrict__ fwi,
                                                       unsigned short* __restrict__ out, char* ws) {
    extern __shared__ __attribute__((aligned(16))) char lds[];
    const int tid = threadIdx.x, lane = tid & 63, wid = __builtin_amdgcn_readfirstlane(tid >> 6);
    const int bid = blockIdx.x, xcd = bid & 7;
    const int hl = (bid >> 3) & (HX - 1), head = (xcd / (HX / 2)) * HX + hl, ti = (xcd & (HX / 2 - 1)) * (32 / HX) + (bid >> 3) / HX;
    const int isv = hl / (HX / 2), pr = (head - hl) / 2 + (hl & (HX / 2 - 1));
    const unsigned lds0 = (unsigned)(uintptr_t)lds;
    const rsrc_t rs = __builtin_amdgcn_make_buffer_rsrc((void*)ws, (short)0, WS_END, 0x00020000);
    LAS unsigned* cnt = (LAS unsigned*)(lds + L_C);
    unsigned genv;
    asm volatile("global_load_dword %0, %1, %2 sc1" : "=v"(genv) : "v"(0), "s"(ws) : "memory");
    if (tid < 64) cnt[tid] = 0u;
    BAR();
    const int otile = xcd * 8 + (bid >> 5), cq = (bid >> 3) & 3;
    const int w = wid & 3;
    const int r = lane & 31, h = lane >> 5;
    unsigned tag;
    if (wid < 4) {
        __builtin_amdgcn_s_setprio(3);
        const char* xsrc = (const char*)x + (size_t)(ti * 128 + w * 32 + (lane >> 3)) * 896;
        u32x4 xt[8];
#pragma unroll
        for (int i = 0; i < 8; ++i) {
            const char* g = xsrc + (i & 3) * (8 * 896) + (5 + (i >> 2)) * 128 + (((lane & 7) ^ ((4 * (i & 3) + (lane >> 4)) & 7)) * 16);
            xt[i] = *(const u32x4*)g;
        }
        const LAS char* xr = (const LAS char*)lds + L_XS + w * 20480 + r * 128;
        const int xk = (r >> 1) & 7;
        const int trow = 8 * h + ((lane & 15) >> 2), tcol = ((lane >> 4) & 1) * 32 + (lane & 3) * 8;
        const LAS char* wr = (const LAS char*)lds + L_WP + trow * 112 + tcol;
        f32x16 acc0 = f32x16{}, acc1 = f32x16{};
        bf16x8 xf[28], wf[8];
        u32x4 wq[7];
        const char* qs = (const char*)wqkv + ((56 * head) & ~15) + (size_t)(w * 112 + (lane >> 2)) * 2688 + (lane & 3) * 16;
        asm volatile("s_waitcnt vmcnt(8)\n\tv_readfirstlane_b32 %0, %1" : "=s"(tag) : "v"(genv) : "memory"); waitc(cnt + C_M + 1, 4u);
        tag += 1u;
#pragma unroll
        for (int kk = 0; kk < 4; ++kk) { wf[2 * kk] = trfrag(wr + kk * 16 * 112, 112); wf[2 * kk + 1] = trfrag(wr + kk * 16 * 112 + 56, 112); }
#pragma unroll
        for (int c = 0; c < 7; ++c) {
#define XRD(C) _Pragma("unroll") for (int kk = 0; kk < 4; ++kk) xf[4 * (C) + kk] = *(const LAS bf16x8*)(xr + ((C) % 5) * 4096 + (((2 * kk + h) ^ xk) * 16));
            if (c == 0) XRD(0)
            if (c == 1) waitc(cnt + C_M + 3, 4u); else if (c == 3) waitc(cnt + C_M + 6, 4u);
            if (c == 3) {
#pragma unroll
                for (int i = 0; i < 8; ++i) *(LAS u32x4*)(lds + L_XS + w * 20480 + i * 1024 + lane * 16) = xt[i];
                FENCE();
            }
            if (c < 6) XRD(c + 1)
            if (c >= 1 && c < 5) { wq[2 * c - 2] = *(const u32x4*)(qs + (2 * c - 2) * (16 * 2688)); if (c < 4) wq[2 * c - 1] = *(const u32x4*)(qs + (2 * c - 1) * (16 * 2688)); }
#define WNEXT if (c < 6) { wf[2 * kk] = trfrag(wr + ((c + 1) * 64 + kk * 16) * 112, 112); wf[2 * kk + 1] = trfrag(wr + ((c + 1) * 64 + kk * 16) * 112 + 56, 112); } SB();
            SB();
            {
#pragma unroll
                for (int kk = 0; kk < 4; ++kk) { acc0 = MFMA32(wf[2 * kk], xf[4 * c + kk], acc0); acc1 = MFMA32(wf[2 * kk + 1], xf[4 * c + kk], acc1); SB(); WNEXT }
            }
        }
        {
            const int h0 = 2 * pr, dst = WS_KV + (((h0 * 64 + ti * 4 + w) * 4 + (isv ? 2 : 0)) * 64 + lane) * 16;
#pragma unroll
            for (int j = 0; j < 2; ++j) {
                const f32x16 a = j ? acc1 : acc0;
                u32x4 f0, f1;
#pragma unroll
                for (int e = 0; e < 4; ++e) { f0[e] = cvtpk(a[2 * e], a[2 * e + 1]); f1[e] = cvtpk(a[8 + 2 * e], a[9 + 2 * e]); }
                if (h) { f1[2] = isv ? 0x00003f80u : 0x3f803f80u; f1[3] = isv ? 0u : 0x00003f80u; }
                if (isv) {
                    LAS char* tb = (LAS char*)lds + L_XS + w * 20480 + 8192 + j * 2048;
                    LAS u32x2* tw_ = (LAS u32x2*)(tb + r * 64 + h * 8);
                    tw_[0] = u32x2{f0[0], f0[1]}; tw_[2] = u32x2{f0[2], f0[3]}; tw_[4] = u32x2{f1[0], f1[1]}; tw_[6] = u32x2{f1[2], f1[3]};
                    WAITL();
                    const LAS char* tr_ = tb + (4 * h + ((lane & 15) >> 2)) * 64 + tcol;
                    f0 = __builtin_bit_cast(u32x4, trfrag(tr_, 128)); f1 = __builtin_bit_cast(u32x4, trfrag(tr_ + 1024, 128));
                    WAITL();
                }
                st128(rs, dst + j * 262144, f0); st128(rs, dst + j * 262144 + 1024, f1);
            }
        }
#pragma unroll
        for (int i = 0; i < 7; ++i) *(LAS u32x4*)(lds + L_XS + w * 20480 + 8192 + i * 1024 + lane * 16) = wq[i];
        arrive(cnt + C_WQ, lane); waitc(cnt + C_WQ, 4u);

        bf16x8 q0, q1;
        {
            const LAS char* qr = (const LAS char*)lds + L_XS + 8192 + trow * 64 + tcol + ((56 * head) & 15);
            f32x16 qa = f32x16{}, qb = f32x16{};
#pragma unroll
            for (int g = 0; g < 4; ++g) {
                if (g == 2) publish(rs, cnt + C_Q, F_KV + (pr * 32 + isv * 16 + ti) * 128, tag, lane, 1);
                bf16x8 wf[7];
#pragma unroll
                for (int kk = 0; kk < 7; ++kk) {
                    wf[kk] = trfrag(qr + g * 20480 + kk * 1024, 64);
                    const int ks = 7 * g + kk;
                    if (ks >= 16) xf[ks] = *(const LAS bf16x8*)(xr + ((ks >> 2) % 5) * 4096 + (((2 * (ks & 3) + h) ^ xk) * 16));
                }
                SB();
#pragma unroll
                for (int kk = 0; kk < 7; ++kk) { if (kk & 1) qb = MFMA32(wf[kk], xf[7 * g + kk], qb); else qa = MFMA32(wf[kk], xf[7 * g + kk], qa); }
            }
            qa = (qa + qb) * QSCALE;
            u32x4 f0, f1;
#pragma unroll
            for (int e = 0; e < 4; ++e) { f0[e] = cvtpk(qa[2 * e], qa[2 * e + 1]); f1[e] = cvtpk(qa[8 + 2 * e], qa[9 + 2 * e]); }
            if (h) { f1[2] = 0u; f1[3] = 0u; }
            q0 = BC8(f0); q1 = BC8(f1);
        }
        LAS bf16x8* qs_ = (LAS bf16x8*)(lds + L_WP) + lane;
        qs_[w * 128] = q0;
        if (w == 0) { poll32(rs, F_KV + ((head >> 1) * 32 + (lane & 31)) * 128, tag); arrive(cnt + C_PK, lane); }
        else waitc(cnt + C_PK, 1u);
        const int kvo = WS_KV + head * 262144 + lane * 16;
        bf16x8 k0, k1, v0, v1, v1p, n0, n1, n2, n3;
        f32x16 sa, sb, o, oo[4];
        {
            n0 = BC8(ld128(rs, kvo)); n1 = BC8(ld128(rs, kvo + 1024));
            const int t0 = kvo + w * 4096;
            k0 = BC8(ld128(rs, t0)); k1 = BC8(ld128(rs, t0 + 1024)); v0 = BC8(ld128(rs, t0 + 2048)); v1 = BC8(ld128(rs, t0 + 3072));
            sa = MFMA32(n0, q0, f32x16{}); sa = MFMA32(n1, q1, sa);
            n0 = BC8(ld128(rs, t0 + 16384)); n1 = BC8(ld128(rs, t0 + 17408)); n2 = BC8(ld128(rs, t0 + 18432)); n3 = BC8(ld128(rs, t0 + 19456));
            q1 = set_ref(q1, xhalf_max(max16(sa)), h);
            qs_[w * 128 + 64] = q1;
            arrive(cnt + C_QS, lane); waitc(cnt + C_QS, 4u);
        }
        bf16x8 qa[4], qb[4];
#pragma unroll
        for (int i = 0; i < 4; ++i) { qa[i] = qs_[i * 128]; qb[i] = qs_[i * 128 + 64]; oo[i] = f32x16{}; }
        __builtin_amdgcn_s_setprio(0);
        sa = MFMA32(k0, qa[0], f32x16{}); sa = MFMA32(k1, qb[0], sa);
        v1p = v1;
        u32x4 p0, p1 = u32x4{};
        float e0 = 0.f, e1 = 0.f, e2 = 0.f, e3 = 0.f;
#define E4(SA, i) e0 = EX(SA[i]); e1 = EX(SA[i + 1]); e2 = EX(SA[i + 2]); e3 = EX(SA[i + 3]); SB();
#define STEPQ(SA, SN, K0, K1, QN, OP, VP, OC, V0) \
        SN = MFMA32(K0, qa[QN], f32x16{}); SB(); p1[2] = cvtpk(e0, e1); p1[3] = cvtpk(e2, e3); SB(); \
        E4(SA, 0) \
        oo[OP] = MFMA32(VP, BC8(p1), oo[OP]); SB(); p0[0] = cvtpk(e0, e1); p0[1] = cvtpk(e2, e3); SB(); \
        E4(SA, 4) \
        SN = MFMA32(K1, qb[QN], SN); SB(); p0[2] = cvtpk(e0, e1); p0[3] = cvtpk(e2, e3); SB(); \
        E4(SA, 8) \
        oo[OC] = MFMA32(V0, BC8(p0), oo[OC]); SB(); p1[0] = cvtpk(e0, e1); p1[1] = cvtpk(e2, e3); SB(); \
        E4(SA, 12)
#define TILE(K0, K1, V0, V1, VP, N0, N1, OFF) \
        STEPQ(sa, sb, K0, K1, 1, 3, VP, 0, V0) \
        VP = BC8(ld128(rs, (OFF) - 16384 + 3072)); SB(); \
        STEPQ(sb, sa, K0, K1, 2, 0, V1, 1, V0) \
        STEPQ(sa, sb, K0, K1, 3, 1, V1, 2, V0) \
        K0 = BC8(ld128(rs, (OFF))); K1 = BC8(ld128(rs, (OFF) + 1024)); SB(); \
        STEPQ(sb, sa, N0, N1, 0, 2, V1, 3, V0) \
        V0 = BC8(ld128(rs, (OFF) + 2048)); SB();
        n3 = v1p;
#pragma unroll 1
        for (int i = 0; i < 8; ++i) {
            const int off = kvo + (((2 * i + 2) * 4 + w) & 63) * 4096;
            const int offb = i == 7 ? off + 64 * 4096 : off;
            STEPQ(sa, sb, k0, k1, 1, 3, n3, 0, v0)
            n3 = BC8(ld128(rs, offb - 16384 + 3072)); SB();
            STEPQ(sb, sa, k0, k1, 2, 0, v1, 1, v0)
            STEPQ(sa, sb, k0, k1, 3, 1, v1, 2, v0)
            k0 = BC8(ld128(rs, off)); k1 = BC8(ld128(rs, off + 1024)); SB();
            STEPQ(sb, sa, n0, n1, 0, 2, v1, 3, v0)
            v0 = BC8(ld128(rs, off + 2048)); SB();
            TILE(n0, n1, n2, n3, v1, k0, k1, off + 16384)
        }
        p1[2] = cvtpk(e0, e1); p1[3] = cvtpk(e2, e3);
        oo[3] = MFMA32(n3, BC8(p1), oo[3]);
        __builtin_amdgcn_s_setprio(3);
        {
            LAS f32x4* ox = (LAS f32x4*)(lds + 16384) + lane;
#pragma unroll
            for (int i = 0; i < 4; ++i) {
                if (i != w) {
#pragma unroll
                    for (int e = 0; e < 4; ++e) ox[((w * 4 + i) * 4 + e) * 64] = f32x4{oo[i][4 * e], oo[i][4 * e + 1], oo[i][4 * e + 2], oo[i][4 * e + 3]};
                } else o = oo[i];
            }
            arrive(cnt + C_OX, lane); waitc(cnt + C_OX, 4u);
#pragma unroll
            for (int i = 1; i < 4; ++i) {
#pragma unroll
                for (int e = 0; e < 4; ++e) { const f32x4 v = ox[((((w + i) & 3) * 4 + w) * 4 + e) * 64]; o[4 * e] += v[0]; o[4 * e + 1] += v[1]; o[4 * e + 2] += v[2]; o[4 * e + 3] += v[3]; }
            }
        }
        float l = hi_half(o[12]);
        float chk = 0.f;
#pragma unroll
        for (int e = 0; e < 16; ++e) chk += o[e] * 0.f;
        if (__builtin_amdgcn_ballot_w64(!(l > 0.25f && l < 1.0e18f && chk == 0.f)) != 0ull) {
            const int g = WS_KV + head * 262144 + lane * 16;
            q1 = set_ref(q1, 0.f, h);
            float m = -INFINITY;
#pragma unroll 1
            for (int t = 0; t < 64; ++t) {
                f32x16 s = MFMA32(BC8(ld128(rs, g + t * 4096)), q0, f32x16{});
                s = MFMA32(BC8(ld128(rs, g + t * 4096 + 1024)), q1, s);
                m = __builtin_fmaxf(m, max16(s));
            }
            q1 = set_ref(q1, xhalf_max(m), h);
            o = f32x16{};
#pragma unroll 1
            for (int t = 0; t < 64; ++t) {
                f32x16 s = MFMA32(BC8(ld128(rs, g + t * 4096)), q0, f32x16{});
                s = MFMA32(BC8(ld128(rs, g + t * 4096 + 1024)), q1, s);
#pragma unroll
                for (int e = 0; e < 4; ++e) { p0[e] = cvtpk(EX(s[2 * e]), EX(s[2 * e + 1])); p1[e] = cvtpk(EX(s[8 + 2 * e]), EX(s[9 + 2 * e])); }
                o = MFMA32(BC8(ld128(rs, g + t * 4096 + 2048)), BC8(p0), o);
                o = MFMA32(BC8(ld128(rs, g + t * 4096 + 3072)), BC8(p1), o);
            }
            l = hi_half(o[12]);
        }
        const float inv = 1.0f / l;
#pragma unroll
        for (int e = 0; e < 4; ++e) { p0[e] = cvtpk(o[2 * e] * inv, o[2 * e + 1] * inv); p1[e] = cvtpk(o[8 + 2 * e] * inv, o[9 + 2 * e] * inv); }
        {
            const int dst = WS_OF + ((((ti * 4 + w) * 16 + head) * 2) * 64 + lane) * 16;
            waitc(cnt + C_R0, 4u);
            st128(rs, dst, p0); st128(rs, dst + 1024, p1);
        }
        publish(rs, cnt + C_O, F_O + (ti * 16 + head) * 128, tag, lane, 1);

    } else {
        __builtin_amdgcn_s_setprio(2);
        const int b = bid, t = tid - 256;
        unsigned wv[8];
        LAS cf* fb = (LAS cf*)(lds + L_F);
        LAS cf* f = fb + w * 7 * RSTR;
        const int l2 = lane < 56 ? lane : 55;
        const int n2 = 2 * t, pos = (n2 >> 6) * RSTR + (n2 & 63);
        const float rr = 0.70710678118654752f;
        const int s2 = l2 >> 3, r0 = l2 & 7, m1 = l2 / 7, s3 = l2 - 7 * m1;
        const int j = 8 * w + (lane >> 3), j0 = (lane >> 3) & 3, d0 = 2 * w + (lane >> 5);
        cf a[8], t448[7], t64[8], t256[8], t32[8], tb1, tb2, tb3, tb4;
        {
            unsigned xv[8];
            const int tc = t < 224 ? t : 223;
#pragma unroll
            for (int i = 0; i < 8; ++i) xv[i] = *(const unsigned*)(bx + (size_t)(256 * i + b) * 448 + 2 * tc);
            {
                const char* wsrc = (const char*)wqkv + 896 * (1 + isv) + 112 * pr;
                const char* xsrc = (const char*)x + (size_t)(ti * 128 + w * 32 + (lane >> 3)) * 896;
                const int pa = 2 * w, pb = w < 3 ? 2 * w + 1 : 6;
                const int ca = pa * 64 + lane, cb_ = pb * 64 + lane;
                const int oa = (ca / 7) * 2688 + (ca % 7) * 16, ob = (cb_ / 7) * 2688 + (cb_ % 7) * 16;
                const int x0 = ((lane & 7) ^ (lane >> 4)) * 16, x1 = ((lane & 7) ^ (4 + (lane >> 4))) * 16;
                auto ldc = [&](int c) __attribute__((always_inline)) {
                    glds16(wsrc + c * (64 * 2688) + oa, lds0 + L_WP + (c * 7 + pa) * 1024);
                    glds16(wsrc + c * (64 * 2688) + ob, lds0 + L_WP + (c * 7 + pb) * 1024);
                    if (c < 5) {
#pragma unroll
                        for (int i = 0; i < 4; ++i) glds16(xsrc + i * (8 * 896) + c * 128 + ((i & 1) ? x1 : x0), lds0 + L_XS + w * 20480 + c * 4096 + i * 1024);
                    }
                };
                ldc(0); ldc(1); ldc(2); ldc(3); ldc(5); ldc(6);
                WAITV(16); arrive(cnt + C_M + 1, lane); ldc(4);
                WAITV(10); arrive(cnt + C_M + 3, lane);
                WAITV(0); arrive(cnt + C_M + 6, lane);
            }
            const int item = bid * 112 + (t < 112 ? t : 111), l = item & 63, fr = item >> 6, hd = fr & 15, cb = fr >> 4;
            const int g = l >> 4, dd = 16 * (g >> 1) + 4 * (g & 1);
            const unsigned short* src = wo + (size_t)(hd * 28) * 448 + cb * 16 + (l & 15);
            if (w < 2) {
#pragma unroll
                for (int e = 0; e < 8; ++e) { const int d = dd + 8 * (e >> 2) + (e & 3); wv[e] = src[(d < 28 ? d : 0) * 448]; }
            }
#pragma unroll
            for (int i = 1; i < 8; ++i) { if (i < 7) t448[i] = tw(lane * i, 1.f / 448); t64[i] = tw(r0 * i, 1.f / 64); t256[i] = tw(j * i, 1.f / 256); t32[i] = tw(j0 * i, 1.f / 32); }
            tb1 = tw(b, 1.f / 2048); tb2 = tw(2 * b, 1.f / 2048); tb3 = tw(3 * b, 1.f / 2048); tb4 = tw(b, 1.f / 512);
#pragma unroll
            for (int i = 1; i < 8; ++i) { if (i < 7) asm volatile("" : "+v"(t448[i])); asm volatile("" : "+v"(t64[i]), "+v"(t256[i]), "+v"(t32[i])); }
            asm volatile("" : "+v"(tb1), "+v"(tb2), "+v"(tb3), "+v"(tb4));
            if (t < 224) {
                cf v[8];
#pragma unroll
                for (int i = 0; i < 8; ++i) v[i] = cf{bflo(xv[i]), bfhi(xv[i])};
                cf p0 = v[0] + v[4], m0 = v[0] - v[4], p1 = v[1] + v[5], n1 = v[1] - v[5];
                cf p2 = v[2] + v[6], m2 = v[2] - v[6], p3 = v[3] + v[7], m3 = v[3] - v[7];
                cf e0 = p0 + p2, e1 = p1 + p3;
                cf z0 = e0 + e1, z4 = e0 - e1;
                cf z2r = p0 - p2, z2i = p3 - p1;
                cf qa = (n1 - m3) * rr, qb = (n1 + m3) * rr;
                cf z1r = m0 + qa, z1i = -m2 - qb, z3r = m0 - qa, z3i = m2 - qb;
                const cf w1 = tb1, w2 = tb2, w3 = tb3;
                cf a0, a1;
                *(LAS f32x4*)(fb + pos) = f32x4{z0.x, z4.x, z0.y, z4.y};
                a0 = cmul<-1>(cf{z1r.x, z1i.x}, w1); a1 = cmul<-1>(cf{z1r.y, z1i.y}, w1);
                *(LAS f32x4*)(fb + 7 * RSTR + pos) = f32x4{a0.x, a0.y, a1.x, a1.y};
                a0 = cmul<-1>(cf{z2r.x, z2i.x}, w2); a1 = cmul<-1>(cf{z2r.y, z2i.y}, w2);
                *(LAS f32x4*)(fb + 14 * RSTR + pos) = f32x4{a0.x, a0.y, a1.x, a1.y};
                a0 = cmul<-1>(cf{z3r.x, z3i.x}, w3); a1 = cmul<-1>(cf{z3r.y, z3i.y}, w3);
                *(LAS f32x4*)(fb + 21 * RSTR + pos) = f32x4{a0.x, a0.y, a1.x, a1.y};
            }
            arrive(cnt + C_B0, lane);
            if (w >= 2 && lane < 40) {
                int e = (8 * (8 * (bid >> 3) + lane / 5) + xcd) * 225 + 7 * xcd + (lane % 5) * 64;
                e = e < 2048 * 225 ? e : 2048 * 225 - 1;
                wv[0] = (w == 2 ? fwr : fwi)[e];
            }
            waitc(cnt + C_B0, 4u);
        }
#pragma unroll
        for (int p = 0; p < 7; ++p) a[p] = f[p * RSTR + lane];
        dft7<-1>(a);
#pragma unroll
        for (int s = 1; s < 7; ++s) a[s] = cmul<-1>(a[s], t448[s]);
#pragma unroll
        for (int s = 0; s < 7; ++s) f[s * RSTR + lane] = a[s];
        WAITL();
#pragma unroll
        for (int i = 0; i < 8; ++i) a[i] = f[s2 * RSTR + 8 * i + r0];
        dft8<-1>(a);

#pragma unroll
        for (int i = 1; i < 8; ++i) a[i] = cmul<-1>(a[i], t64[i]);
        if (lane < 56) {
#pragma unroll
            for (int i = 0; i < 8; ++i) f[s2 * RSTR + 8 * i + r0] = a[i];
        }
        WAITL();
#pragma unroll
        for (int i = 0; i < 8; ++i) a[i] = f[s3 * RSTR + 8 * m1 + i];
        dft8<-1>(a);
        {
            LAS cf* y = (LAS cf*)(lds + L_YA);
            if (w != 0) {
                if (lane < 56) {
#pragma unroll
                    for (int i = 0; i < 4; ++i) y[w * 225 + lane + 56 * i] = a[i];
#pragma unroll
                    for (int i = 4; i < 8; ++i) y[(8 - w) * 225 + 448 - lane - 56 * i] = cconj(a[i]);
                    if (lane == 0) { y[w * 225 + 224] = a[4]; y[(8 - w) * 225] = cconj(a[0]); }
                }
            } else {
                WAITL();
                if (lane < 56) {
#pragma unroll
                    for (int i = 0; i < 8; ++i) f[lane + 56 * i] = a[i];
                }
                if (lane == 0) f[448] = a[0];
                WAITL();
                const cf w4 = cf{tb4.y, -tb4.x} * 0.5f;
#pragma unroll
                for (int i = 0; i < 4; ++i) {
                    const int k = lane + 64 * i;
                    if (k <= 224) {
                        const cf u = f[k], v = cconj(f[448 - k]);
                        y[k] = (u + v) * 0.5f;
                        y[4 * 225 + k] = cmul<-1>(u - v, w4);
                    }
                }
            }
            arrive(cnt + C_B1, lane); waitc(cnt + C_B1, 4u);
#pragma unroll
            for (int i = 0; i < 4; ++i) { const int e = t + 256 * i; if (e < 900) st128(rs, WS_Y1 + b * (NQ * 8) + e * 16, ((LAS u32x4*)y)[e]); }
            const bool pad = ((bid * 112 + t) & 48) == 48;
            if (t < 112) st128(rs, WS_WO + (bid * 112 + t) * 16, u32x4{wv[0] | (wv[1] << 16), wv[2] | (wv[3] << 16), pad ? 0u : wv[4] | (wv[5] << 16), pad ? 0u : wv[6] | (wv[7] << 16)});
        }
        asm volatile("s_waitcnt vmcnt(0)\n\tv_readfirstlane_b32 %0, %1" : "=s"(tag) : "v"(genv) : "memory");
        asm volatile("" :: "v"(wv[0]));
        tag += 1u;
        publish(rs, cnt + C_B3, F_Y1 + bid * 4, tag, lane, 8);
        waitc(cnt + C_PK, 1u);
        {
            const int grp = (bid & 7) * 29 + (bid >> 3);
            const bool act = (bid >> 3) < 29 && grp < 225;
            const int col = lane & 7, q = (act ? grp : 0) * 8 + col, c = q / 225, k2 = q - c * 225, cs = c > 4 ? c - 8 : c;
            {
                const int dst = WS_OF + ((((ti * 4 + w) * 16 + head) * 2) * 64 + lane) * 16;
                st128(rs, dst, u32x4{}); st128(rs, dst + 1024, u32x4{});
            }
            unsigned fr_[8], fi_[8];
#pragma unroll
            for (int i = 0; i < 8; ++i) {
                const int d = d0 + 8 * (j0 + 4 * (i >> 2)) + 64 * (i & 3), k1 = (cs + 8 * d) & 2047;
                fr_[i] = fwr[k1 * 225 + k2]; fi_[i] = fwi[k1 * 225 + k2];
                __builtin_amdgcn_s_sleep(3); FENCE();
            }
            if (w == 0) {
                poll128(rs, F_Y1 + xcd * FREP + lane * 16, tag);
                arrive(cnt + C_B2, lane);
            } else waitc(cnt + C_B2, 1u);
            if (t < 112) { wv[0] = ld32(rs, WS_WO + ((bid >> 3) * 112 + t) * 128); asm volatile("" :: "v"(wv[0])); }
            LAS cf* mb = fb;
#pragma unroll
            for (int i = 0; i < 8; ++i) a[i] = ld64(rs, WS_Y1 + ((32 * i + j) * NQ + q) * 8);
            if (act) {
                dft8<-1>(a);
#pragma unroll
                for (int i = 1; i < 8; ++i) a[i] = cmul<-1>(a[i], t256[i]);
#pragma unroll
                for (int i = 0; i < 8; ++i) mb[(i * 32 + j) * 8 + col] = a[i];
                arrive(cnt + C_B4, lane); waitc(cnt + C_B4, 4u);
#pragma unroll
                for (int i = 0; i < 8; ++i) a[i] = mb[(d0 * 32 + 4 * i + j0) * 8 + col];
                dft8<-1>(a);
#pragma unroll
                for (int i = 1; i < 8; ++i) a[i] = cmul<-1>(a[i], t32[i]);
#pragma unroll
                for (int i = 0; i < 8; ++i) mb[(d0 * 32 + 4 * i + j0) * 8 + col] = a[i];
                WAITL();
#pragma unroll
                for (int i = 0; i < 8; ++i) a[i] = mb[(d0 * 32 + 4 * (j0 + 4 * (i >> 2)) + (i & 3)) * 8 + col];
                dft4<-1>(a[0], a[1], a[2], a[3]); dft4<-1>(a[4], a[5], a[6], a[7]);
#pragma unroll
                for (int i = 0; i < 8; ++i) a[i] = cmul<1>(a[i], cf{bflo(fi_[i]), bflo(fr_[i])});
                dft4<1>(a[0], a[1], a[2], a[3]); dft4<1>(a[4], a[5], a[6], a[7]);
#pragma unroll
                for (int i = 0; i < 8; ++i) mb[(d0 * 32 + 4 * (j0 + 4 * (i >> 2)) + (i & 3)) * 8 + col] = a[i];
                WAITL();
#pragma unroll
                for (int i = 0; i < 8; ++i) a[i] = mb[(d0 * 32 + 4 * i + j0) * 8 + col];
#pragma unroll
                for (int i = 1; i < 8; ++i) a[i] = cmul<1>(a[i], t32[i]);
                dft8<1>(a);
#pragma unroll
                for (int i = 0; i < 8; ++i) mb[(d0 * 32 + 4 * i + j0) * 8 + col] = a[i];
                arrive(cnt + C_B5, lane); waitc(cnt + C_B5, 4u);
#pragma unroll
                for (int i = 0; i < 8; ++i) a[i] = mb[(i * 32 + j) * 8 + col];
#pragma unroll
                for (int i = 1; i < 8; ++i) a[i] = cmul<1>(a[i], t256[i]);
                dft8<1>(a);
#pragma unroll
                for (int i = 0; i < 8; ++i) st64(rs, WS_Y2 + ((32 * i + j) * NQ + q) * 8, a[i]);
            }
        }
        publish(rs, cnt + C_R0, F_Y2 + bid * 4, tag, lane, 8);
        bf16x8 wa[16], wb[16];
        const int cb0 = cq * 7 + w, cb1 = cq * 7 + (w < 3 ? w + 4 : 6);
#pragma unroll
        for (int i = 0; i < 16; ++i) { wa[i] = BC8(ld128(rs, WS_WO + ((cb0 * 16 + i) * 64 + lane) * 16)); wb[i] = BC8(ld128(rs, WS_WO + ((cb1 * 16 + i) * 64 + lane) * 16)); __builtin_amdgcn_s_sleep(5); FENCE(); }
        waitc(cnt + C_R0, 4u);
        if (w == 0) {
            poll128(rs, F_Y2 + xcd * FREP + lane * 16, tag);
            arrive(cnt + C_PY, lane);
            if (bid == 0 && lane == 0) st32(rs, F_GEN, tag);
        } else waitc(cnt + C_PY, 1u);
        {
            const int y = WS_Y2 + b * (NQ * 8);
            cf z[8];
            const int ya = y + (w * 225) * 8, yb = y + ((w ? 8 - w : 0) * 225) * 8;
#pragma unroll
            for (int i = 0; i < 8; ++i) a[i] = ld64(rs, i < 4 ? ya + (l2 + 56 * i) * 8 : yb + (448 - l2 - 56 * i) * 8);
            if (w) { z[0] = ld64(rs, yb); z[4] = ld64(rs, ya + 224 * 8); }
            else {
#pragma unroll
                for (int i = 0; i < 8; ++i) z[i] = ld64(rs, y + (900 + (i < 4 ? l2 + 56 * i : 448 - l2 - 56 * i)) * 8);
            }
            waitc(cnt + C_OX, 1u);
#pragma unroll
            for (int i = 4; i < 8; ++i) a[i] = cconj(a[i]);
            if (w) { if (lane == 0) { a[0] = (a[0] + cconj(z[0])) * 0.5f; a[4] = (a[4] + z[4]) * 0.5f; } }
            else {
                const cf w4 = tb4;
#pragma unroll
                for (int i = 0; i < 8; ++i) {
                    cf z4 = cmul<1>(z[i], w4);
                    if (i >= 4) z4 = cconj(z4);
                    if ((i & 3) == 0 && lane == 0) { a[i].y = 0.f; z4.y = 0.f; }
                    a[i] = addrot<1>(a[i], z4);
                }
            }
        }
        dft8<1>(a);
        if (lane < 56) {
#pragma unroll
            for (int i = 0; i < 8; ++i) f[s3 * RSTR + 8 * m1 + i] = a[i];
        }
        WAITL();
#pragma unroll
        for (int i = 0; i < 8; ++i) a[i] = f[s2 * RSTR + 8 * i + r0];
#pragma unroll
        for (int i = 1; i < 8; ++i) a[i] = cmul<1>(a[i], t64[i]);
        dft8<1>(a);
        if (lane < 56) {
#pragma unroll
            for (int i = 0; i < 8; ++i) f[s2 * RSTR + 8 * i + r0] = a[i];
        }
        WAITL();
#pragma unroll
        for (int s = 0; s < 7; ++s) a[s] = f[s * RSTR + lane];
#pragma unroll
        for (int s = 1; s < 7; ++s) a[s] = cmul<1>(a[s], t448[s]);
        dft7<1>(a);
#pragma unroll
        for (int p = 0; p < 7; ++p) f[p * RSTR + lane] = a[p];
        arrive(cnt + C_B6, lane); waitc(cnt + C_B6, 4u);
        if (t < 224) {
            const float sc = 1.0f / (2048.0f * 448.0f), sc2 = 2.f * sc;
            const f32x4 u0 = *(LAS f32x4*)(fb + pos), u1 = *(LAS f32x4*)(fb + 7 * RSTR + pos), u2 = *(LAS f32x4*)(fb + 14 * RSTR + pos), u3 = *(LAS f32x4*)(fb + 21 * RSTR + pos);
            const cf w1 = tb1 * sc2, w2 = tb2 * sc2, w3 = tb3 * sc2;
            const cf z0 = cf{u0[0], u0[2]} * sc, z4 = cf{u0[1], u0[3]} * sc;
            cf a0 = cmul<1>(cf{u1[0], u1[1]}, w1), a1 = cmul<1>(cf{u1[2], u1[3]}, w1);
            const cf z1r = cf{a0.x, a1.x}, z1i = cf{a0.y, a1.y};
            a0 = cmul<1>(cf{u2[0], u2[1]}, w2); a1 = cmul<1>(cf{u2[2], u2[3]}, w2);
            const cf z2r = cf{a0.x, a1.x}, z2i = cf{a0.y, a1.y};
            a0 = cmul<1>(cf{u3[0], u3[1]}, w3); a1 = cmul<1>(cf{u3[2], u3[3]}, w3);
            const cf z3r = cf{a0.x, a1.x}, z3i = cf{a0.y, a1.y};
            const cf e0 = z0 + z4, e1 = z0 - z4;
            const cf p0 = e0 + z2r, p2 = e0 - z2r, p1 = e1 - z2i, p3 = e1 + z2i;
            const cf m0 = z1r + z3r, m2 = z3i - z1i;
            const cf qa = (z1r - z3r) * rr, qb = (z1i + z3i) * rr;
            const cf mm1 = qa - qb, m3 = -qa - qb;
            const cf yv[8] = {p0 + m0, p1 + mm1, p2 + m2, p3 + m3, p0 - m0, p1 - mm1, p2 - m2, p3 - m3};
#pragma unroll
            for (int i = 0; i < 8; ++i) *(unsigned*)(out + (size_t)(256 * i + b) * 896 + 448 + n2) = cvtpk(yv[i].x, yv[i].y);
        }
        waitc(cnt + C_OX, 4u);
        __builtin_amdgcn_s_setprio(3);
        if (w == 0) { poll32(rs, F_O + ((otile >> 2) * 16 + (lane & 15)) * 128, tag); arrive(cnt + C_PO, lane); }
        else waitc(cnt + C_PO, 1u);
        u32x4 ov[8];
#pragma unroll
        for (int i = 0; i < 8; ++i) ov[i] = ld128(rs, WS_OF + (otile * 2048 + (i * 4 + w) * 64 + lane) * 16);
#pragma unroll
        for (int i = 0; i < 8; ++i) ((LAS u32x4*)(lds + 98304))[(i * 4 + w) * 64 + lane] = ov[i];
        arrive(cnt + C_OS, lane); waitc(cnt + C_OS, 4u);
        {
            const int g = lane >> 4;
            const LAS bf16x8* op = (const LAS bf16x8*)(lds + 98304) + (g >> 1) * 64 + (g & 1) * 32 + (lane & 15);
            f32x4 c0 = f32x4{}, c1 = f32x4{}, c2 = f32x4{}, c3 = f32x4{};
#pragma unroll
            for (int i = 0; i < 16; ++i) {
                const bf16x8 a0 = op[i * 128], a1 = op[i * 128 + 16];
                c0 = MFMA16(wa[i], a0, c0); c1 = MFMA16(wa[i], a1, c1); c2 = MFMA16(wb[i], a0, c2); c3 = MFMA16(wb[i], a1, c3);
            }
            unsigned short* dst = out + (size_t)(otile * 32 + (lane & 15)) * 896 + g * 4;
            *(u32x2*)(dst + cb0 * 16) = u32x2{cvtpk(c0[0], c0[1]), cvtpk(c0[2], c0[3])};
            *(u32x2*)(dst + cb0 * 16 + 16 * 896) = u32x2{cvtpk(c1[0], c1[1]), cvtpk(c1[2], c1[3])};
            if (w < 3) {
                *(u32x2*)(dst + cb1 * 16) = u32x2{cvtpk(c2[0], c2[1]), cvtpk(c2[2], c2[3])};
                *(u32x2*)(dst + cb1 * 16 + 16 * 896) = u32x2{cvtpk(c3[0], c3[1]), cvtpk(c3[2], c3[3])};
            }
        }
    }
}

extern "C" void kernel_launch(void* const* d_in, const int* in_sizes, int n_in, void* d_out, int out_size, void* d_ws,
                              size_t ws_size, hipStream_t stream) {
    (void)hipFuncSetAttribute((const void*)fused_kernel, hipFuncAttributeMaxDynamicSharedMemorySize, L_END);
    fused_kernel<<<256, 512, L_END, stream>>>((const unsigned short*)d_in[0], (const unsigned short*)d_in[1], (const unsigned short*)d_in[2],
                                              (const unsigned short*)d_in[3], (const unsigned short*)d_in[4], (const unsigned short*)d_in[5],
                                              (unsigned short*)d_out, (char*)d_ws);
}
```
